```python
import torch

def _softmax(x, axis=-1):
    return torch.softmax(x, dim=axis)
def _gelu(x):
    return torch.nn.functional.gelu(x, approximate="tanh")
def _silu(x):
    return torch.nn.functional.silu(x)
def _sigmoid(x):
    return torch.sigmoid(x)
def _layernorm(x, axis=-1, eps=1e-5):
    m = x.mean(dim=axis, keepdim=True)
    v = x.var(dim=axis, keepdim=True, unbiased=False)
    return (x - m) / torch.sqrt(v + eps)
def _scatter_add(tgt, idx, src, axis=0):
    return tgt.clone().index_add_(axis, idx, src)
def _segment_sum(src, seg, n):
    out = torch.zeros((n,) + src.shape[1:], dtype=src.dtype)
    return out.index_add_(0, seg, src)
def _pad(x, pw):
    flat = tuple(v for pair in reversed(pw) for v in pair)
    return torch.nn.functional.pad(x, flat)

kernel_name = 'concat_heads__mha_block__spectral_fnet__prompt'

def setup_inputs():

    _w = (lambda a: (lambda t: t.to(torch.bfloat16) if t.is_floating_point() else t)(torch.as_tensor(a)))
    def _A_setup():
        B, S, D, H = 1, 2048, 512, 32
        return {
            "x": _w((lambda *s: torch.randn(*s, dtype=torch.bfloat16) * 0.2)(B, S, D)),
            "wqkv": _w((lambda *s: torch.randn(*s, dtype=torch.bfloat16) * 0.8)(D, 3 * D)),
            "wo": _w((lambda *s: torch.randn(*s, dtype=torch.bfloat16) * 0.2)(D, D)),
        }
    _A_in = _A_setup()

    def _B_setup():
        B, S, D = 1, 2048, 512
        ns = torch.arange(S)[:, None] * torch.arange(S)[None, :]
        nd = torch.arange(D)[:, None] * torch.arange(D)[None, :]
        Ws_re = torch.cos(-2.0 * torch.pi * ns / S).to(float)
        Ws_im = torch.sin(-2.0 * torch.pi * ns / S).to(float)
        Wd_re = torch.cos(-2.0 * torch.pi * nd / D).to(float)
        Wd_im = torch.sin(-2.0 * torch.pi * nd / D).to(float)
        return {
            "x": _w((lambda *s: torch.randn(*s, dtype=torch.bfloat16) * 0.05)(B, S, D)),
            "dft_seq_re": _w(Ws_re),
            "dft_seq_im": _w(Ws_im),
            "dft_hid_re": _w(Wd_re),
            "dft_hid_im": _w(Wd_im),
        }
    _B_in = _B_setup()

    return {
        "A_x": _A_in["x"],
        "A_wqkv": _A_in["wqkv"],
        "A_wo": _A_in["wo"],
        "B_x": _B_in["x"],
        "B_dft_seq_re": _B_in["dft_seq_re"],
        "B_dft_seq_im": _B_in["dft_seq_im"],
        "B_dft_hid_re": _B_in["dft_hid_re"],
        "B_dft_hid_im": _B_in["dft_hid_im"],
    }

def reference(A_x, A_wqkv, A_wo, B_x, B_dft_seq_re, B_dft_seq_im, B_dft_hid_re, B_dft_hid_im):
    def _A_ref(x, wqkv, wo):
        B, S, D = x.shape
        H = 32
        Dh = D // H
        qkv = (lambda x, *d: x.permute(*d))((x @ wqkv).reshape(B, S, 3, H, Dh), 2, 0, 3, 1, 4)
        q, k, v = qkv[0], qkv[1], qkv[2]
        scores = torch.einsum("bhqd,bhkd->bhqk", q, k) * (Dh ** -0.5)
        p = _softmax(scores, axis=-1)
        o = torch.einsum("bhqk,bhkd->bhqd", p, v)
        return (lambda x, *d: x.permute(*d))(o, 0, 2, 1, 3).reshape(B, S, D) @ wo

    def _B_ref(x, dft_seq_re, dft_seq_im, dft_hid_re, dft_hid_im):
        a = torch.einsum("bsd,ed->bse", x, dft_hid_re)
        b = torch.einsum("bsd,ed->bse", x, dft_hid_im)
        y = torch.einsum("ts,bse->bte", dft_seq_re, a) - torch.einsum("ts,bse->bte", dft_seq_im, b)
        return y

    a = _A_ref(A_x, A_wqkv, A_wo)
    b = _B_ref(B_x, B_dft_seq_re, B_dft_seq_im, B_dft_hid_re, B_dft_hid_im)
    return torch.concatenate([a, b], axis=-1)
```

```cpp
#include <hip/hip_runtime.h>
#include <hip/hip_bf16.h>
typedef __hip_bfloat16 bf16;
constexpr int S = 2048, D = 512, H = 32, DH = 16;

__device__ inline float b2f(bf16 v) { return __bfloat162float(v); }

template <bool BT>
__global__ void gemm_naive(const bf16* A, const bf16* B, float* C, int M, int N, int K, int ldc, float alpha, int accumulate) {
  __shared__ float As[16][17], Bs[16][17];
  int tx = threadIdx.x & 15, ty = threadIdx.x >> 4;
  int row = blockIdx.y * 16 + ty, col = blockIdx.x * 16 + tx;
  float acc = 0;
  for (int k0 = 0; k0 < K; k0 += 16) {
    As[ty][tx] = b2f(A[(size_t)row * K + k0 + tx]);
    if (BT) Bs[tx][ty] = b2f(B[(size_t)(blockIdx.x * 16 + ty) * K + k0 + tx]);
    else Bs[ty][tx] = b2f(B[(size_t)(k0 + ty) * N + col]);
    __syncthreads();
    for (int k = 0; k < 16; ++k) acc += As[ty][k] * Bs[k][tx];
    __syncthreads();
  }
  float* p = C + (size_t)row * ldc + col;
  *p = accumulate ? *p + alpha * acc : alpha * acc;
}
__global__ void f2b(const float* a, bf16* b, int n) { int i = blockIdx.x * 256 + threadIdx.x; if (i < n) b[i] = __float2bfloat16(a[i]); }

__global__ void attn_naive(const float* qkv, bf16* o) {
  int q = blockIdx.x * 64 + threadIdx.x, h = blockIdx.y;
  float qv[DH], acc[DH];
  for (int d = 0; d < DH; ++d) { qv[d] = b2f(__float2bfloat16(qkv[(size_t)q * 1536 + h * DH + d])) * 0.25f; acc[d] = 0; }
  float m = -1e30f, l = 0;
  for (int k = 0; k < S; ++k) {
    float s = 0;
    for (int d = 0; d < DH; ++d) s += qv[d] * b2f(__float2bfloat16(qkv[(size_t)k * 1536 + 512 + h * DH + d]));
    float mn = fmaxf(m, s), a = __expf(m - mn), p = __expf(s - mn);
    l = l * a + p;
    for (int d = 0; d < DH; ++d) acc[d] = acc[d] * a + p * b2f(__float2bfloat16(qkv[(size_t)k * 1536 + 1024 + h * DH + d]));
    m = mn;
  }
  for (int d = 0; d < DH; ++d) o[(size_t)q * D + h * DH + d] = __float2bfloat16(acc[d] / l);
}
__global__ void write_out(const float* a, const float* b, bf16* out) {
  int i = blockIdx.x * 256 + threadIdx.x;
  int r = i >> 9, c = i & 511;
  out[(size_t)r * 1024 + c] = __float2bfloat16(a[i]);
  out[(size_t)r * 1024 + 512 + c] = __float2bfloat16(b[i]);
}

extern "C" void kernel_launch(void* const* d_in, const int* in_sizes, int n_in, void* d_out, int out_size, void* d_ws, size_t ws_size, hipStream_t stream) {
  const bf16* ax = (const bf16*)d_in[0]; const bf16* wqkv = (const bf16*)d_in[1]; const bf16* wo = (const bf16*)d_in[2];
  const bf16* bx = (const bf16*)d_in[3]; const bf16* sre = (const bf16*)d_in[4]; const bf16* sim = (const bf16*)d_in[5];
  const bf16* hre = (const bf16*)d_in[6]; const bf16* him = (const bf16*)d_in[7];
  char* ws = (char*)d_ws;
  float* qkv = (float*)ws; ws += (size_t)S * 1536 * 4;
  bf16* o = (bf16*)ws; ws += (size_t)S * D * 2;
  float* ya = (float*)ws; ws += (size_t)S * D * 4;
  float* ta = (float*)ws; ws += (size_t)S * D * 4;
  float* tb = (float*)ws; ws += (size_t)S * D * 4;
  bf16* ab = (bf16*)ws; ws += (size_t)S * D * 2;
  bf16* bb = (bf16*)ws; ws += (size_t)S * D * 2;
  float* yb = (float*)ws; ws += (size_t)S * D * 4;
  gemm_naive<false><<<dim3(1536 / 16, S / 16), 256, 0, stream>>>(ax, wqkv, qkv, S, 1536, D, 1536, 1.f, 0);
  attn_naive<<<dim3(S / 64, H), 64, 0, stream>>>(qkv, o);
  gemm_naive<false><<<dim3(D / 16, S / 16), 256, 0, stream>>>(o, wo, ya, S, D, D, D, 1.f, 0);
  gemm_naive<true><<<dim3(D / 16, S / 16), 256, 0, stream>>>(bx, hre, ta, S, D, D, D, 1.f, 0);
  gemm_naive<true><<<dim3(D / 16, S / 16), 256, 0, stream>>>(bx, him, tb, S, D, D, D, 1.f, 0);
  f2b<<<S * D / 256, 256, 0, stream>>>(ta, ab, S * D);
  f2b<<<S * D / 256, 256, 0, stream>>>(tb, bb, S * D);
  gemm_naive<false><<<dim3(D / 16, S / 16), 256, 0, stream>>>(sre, ab, yb, S, D, S, D, 1.f, 0);
  gemm_naive<false><<<dim3(D / 16, S / 16), 256, 0, stream>>>(sim, bb, yb, S, D, S, D, -1.f, 1);
  write_out<<<S * D / 256, 256, 0, stream>>>(ya, yb, (bf16*)d_out);
}
```

```cpp
#include <hip/hip_runtime.h>
#include <hip/hip_bf16.h>
#include <stdint.h>

typedef __attribute__((ext_vector_type(8))) short bf16x8;
typedef __attribute__((ext_vector_type(4))) short s16x4;
typedef __attribute__((ext_vector_type(16))) float f32x16;
typedef __attribute__((ext_vector_type(4))) unsigned u32x4;
typedef __attribute__((ext_vector_type(2))) unsigned u32x2;
typedef __attribute__((ext_vector_type(4))) float f32x4;
#define LAS __attribute__((address_space(3)))
typedef LAS const char* lds_cptr;

#define SBAR() __builtin_amdgcn_sched_barrier(0)
#define PIN(x) asm volatile("" : "+v"(x))
#define MFMA(a, b, c) __builtin_amdgcn_mfma_f32_32x32x16_bf16(a, b, c, 0, 0, 0)

constexpr int S = 2048, DM = 512, NH = 32;
constexpr size_t WS_Q = 0;
constexpr size_t WS_K = 2u << 20;
constexpr size_t WS_V = 4u << 20;
constexpr size_t WS_AB = 6u << 20;
constexpr size_t WS_O = 10u << 20;
constexpr size_t WS_P = 12u << 20;
#define STEPSTAMP(n)
#define STAMP(kid, i)
#define STAMPC(kid, i)

typedef __attribute__((ext_vector_type(2))) float f32x2;
typedef __attribute__((ext_vector_type(2))) __bf16 bf16x2;
__device__ __forceinline__ unsigned cvtpk(float lo, float hi) { const f32x2 v = {lo, hi}; return __builtin_bit_cast(unsigned, __builtin_convertvector(v, bf16x2)); }
__device__ __forceinline__ void dma16(const void* sbase, unsigned voff, unsigned ldsaddr) {
  asm volatile("s_mov_b32 m0, %2\n\ts_nop 0\n\tglobal_load_lds_dwordx4 %0, %1" ::"v"(voff), "s"(sbase), "s"(ldsaddr) : "memory");
}
#define WAIT_BAR(N) asm volatile("s_waitcnt vmcnt(" #N ") lgkmcnt(0)\n\ts_barrier" ::: "memory")
__device__ __forceinline__ bf16x8 ldsfrag(lds_cptr p) { return *(LAS const bf16x8*)p; }
typedef short v4i16_t __attribute__((ext_vector_type(4)));
__device__ __forceinline__ s16x4 ldstr(lds_cptr p) { return __builtin_bit_cast(s16x4, __builtin_amdgcn_ds_read_tr16_b64_v4i16((LAS v4i16_t*)p)); }
__device__ __forceinline__ bf16x8 cat8(s16x4 a, s16x4 b) { return (bf16x8){a[0], a[1], a[2], a[3], b[0], b[1], b[2], b[3]}; }
__device__ __forceinline__ u32x4 pack8(const f32x16& c, int r0, float sc) {
  return (u32x4){cvtpk(c[r0] * sc, c[r0 + 1] * sc), cvtpk(c[r0 + 2] * sc, c[r0 + 3] * sc), cvtpk(c[r0 + 4] * sc, c[r0 + 5] * sc), cvtpk(c[r0 + 6] * sc, c[r0 + 7] * sc)};
}
__device__ __forceinline__ u32x4 pack8(const f32x16& c, int r0) {
  return (u32x4){cvtpk(c[r0], c[r0 + 1]), cvtpk(c[r0 + 2], c[r0 + 3]), cvtpk(c[r0 + 4], c[r0 + 5]), cvtpk(c[r0 + 6], c[r0 + 7])};
}
__device__ __forceinline__ u32x4 natorder(u32x4 w) {
  auto a = __builtin_amdgcn_permlane32_swap(w[0], w[2], false, false);
  auto b = __builtin_amdgcn_permlane32_swap(w[1], w[3], false, false);
  return (u32x4){a[0], b[0], a[1], b[1]};
}

constexpr int K1_SLOT = 40960, K1_LDS = 4 * K1_SLOT;
__global__ void __launch_bounds__(512, 2) k1_proj(const char* __restrict__ xa, const char* __restrict__ wqkv, const char* __restrict__ xb,
                                                  const char* __restrict__ dre, const char* __restrict__ dim, char* __restrict__ ws) {
  extern __shared__ __attribute__((aligned(16))) char lds[];
  const int lane = threadIdx.x & 63, w = __builtin_amdgcn_readfirstlane(threadIdx.x >> 6), hi = lane >> 5;
  const int x = blockIdx.x & 7, k = blockIdx.x >> 3;
  const int rb = (x & 3) * 4 + (k & 3), cb = (x >> 2) * 4 + ((k >> 2) & 3);
  const unsigned lds0 = (unsigned)(uintptr_t)lds;
  const int rot = (k + x) & 7;
  STAMP(0, 0);
  unsigned vx[2];
#pragma unroll
  for (int j = 0; j < 2; ++j) { const int row = 16 * w + 8 * j + (lane >> 3); vx[j] = row * 1024 + (((lane & 7) ^ ((row >> 1) & 7)) << 4); }
  const int rg = w & 3, ch = w >> 2;
  const int frow = 32 * rg + (lane & 31);
  const lds_cptr xf0 = (lds_cptr)lds + frow * 128 + ((hi ^ ((frow >> 1) & 7)) << 4);
  if (k < 16) {
    const char* xs = xa + (size_t)rb * 128 * 1024;
    const char* wsrc = wqkv + cb * 128;
    const int wr = 8 * w + (lane >> 3);
    const unsigned vw = wr * 3072 + (((lane & 7) ^ (((wr >> 1) & 1) << 2)) << 4);
    auto issue = [&](int s) {
      const int ks = (s + rot) & 7;
      const unsigned sl = lds0 + (s & 3) * K1_SLOT;
      dma16(xs + ks * 128, vx[0], sl + w * 2048);
      dma16(xs + ks * 128, vx[1], sl + w * 2048 + 1024);
      const char* wp = wsrc + (size_t)ks * 64 * 3072;
      dma16(wp, vw, sl + 16384 + w * 1024);
      dma16(wp + 1024, vw, sl + 16384 + 8192 + w * 1024);
      dma16(wp + 2048, vw, sl + 16384 + 16384 + w * 1024);
    };
    STAMPC(0, 8);
    issue(0); issue(1); issue(2);
    const int q4 = (lane & 15) >> 2, p4 = lane & 3, blk = (lane >> 4) & 1;
    const lds_cptr wf0 = (lds_cptr)lds + 16384 + (8 * hi + q4) * 128 + (((4 * ch + 2 * blk + (p4 >> 1)) ^ ((q4 >> 1) << 2)) << 4) + (p4 & 1) * 8;
    f32x16 aq = {}, ak = {}, av = {};
    for (int s = 0; s < 8; ++s) {
      if (s < 6) WAIT_BAR(10); else if (s == 6) WAIT_BAR(5); else WAIT_BAR(0);
      if (s + 3 < 8) issue(s + 3);
      const int so = (s & 3) * K1_SLOT;
#pragma unroll
      for (int ks = 0; ks < 4; ++ks) {
        const bf16x8 xf = ldsfrag((lds_cptr)(((unsigned)(uintptr_t)xf0 ^ (ks << 5)) + so));
        const lds_cptr wp = wf0 + so + ks * 2048;
        const bf16x8 wq = cat8(ldstr(wp), ldstr(wp + 512));
        const bf16x8 wk = cat8(ldstr(wp + 8192), ldstr(wp + 8192 + 512));
        const bf16x8 wv = cat8(ldstr(wp + 16384), ldstr(wp + 16384 + 512));
        aq = MFMA(wq, xf, aq);
        ak = MFMA(wk, xf, ak);
        av = MFMA(xf, wv, av);
      }
    }
    STAMP(0, 1); STAMPC(0, 9);
    const int tile = rb * 4 + rg, h0 = 4 * cb + 2 * ch;
    constexpr float QS = 0.25f * 1.4426950408889634f;
    *(u32x4*)(ws + WS_Q + ((size_t)(h0 * 64 + tile) << 10) + lane * 16) = pack8(aq, 0, QS);
    *(u32x4*)(ws + WS_Q + ((size_t)((h0 + 1) * 64 + tile) << 10) + lane * 16) = pack8(aq, 8, QS);
    *(u32x4*)(ws + WS_K + ((size_t)(h0 * 64 + tile) << 10) + lane * 16) = pack8(ak, 0);
    *(u32x4*)(ws + WS_K + ((size_t)((h0 + 1) * 64 + tile) << 10) + lane * 16) = pack8(ak, 8);
    char* vp = ws + WS_V + ((size_t)(h0 + ((lane >> 4) & 1)) << 16) + tile * 1024 + (hi * 16 + (lane & 15)) * 16;
    *(u32x4*)(vp) = pack8(av, 0);
    *(u32x4*)(vp + 512) = pack8(av, 8);
  } else {
    const char* xs = xb + (size_t)rb * 128 * 1024;
    const char* ds = ((cb >> 2) ? dim : dre) + (size_t)(cb & 3) * 128 * 1024;
    auto issue = [&](int s) {
      const int ks = (s + rot) & 7;
      const unsigned sl = lds0 + (s & 3) * K1_SLOT;
      dma16(xs + ks * 128, vx[0], sl + w * 2048);
      dma16(xs + ks * 128, vx[1], sl + w * 2048 + 1024);
      dma16(ds + ks * 128, vx[0], sl + 16384 + w * 2048);
      dma16(ds + ks * 128, vx[1], sl + 16384 + w * 2048 + 1024);
    };
    issue(0); issue(1); issue(2);
    const int er0 = 64 * ch + (lane & 31), er1 = er0 + 32;
    const lds_cptr df0 = (lds_cptr)lds + 16384 + er0 * 128 + ((hi ^ ((er0 >> 1) & 7)) << 4);
    const lds_cptr df1 = (lds_cptr)lds + 16384 + er1 * 128 + ((hi ^ ((er1 >> 1) & 7)) << 4);
    f32x16 a0 = {}, a1 = {};
    for (int s = 0; s < 8; ++s) {
      if (s < 6) WAIT_BAR(8); else if (s == 6) WAIT_BAR(4); else WAIT_BAR(0);
      if (s + 3 < 8) issue(s + 3);
      const int so = (s & 3) * K1_SLOT;
#pragma unroll
      for (int ks = 0; ks < 4; ++ks) {
        const bf16x8 xf = ldsfrag((lds_cptr)(((unsigned)(uintptr_t)xf0 ^ (ks << 5)) + so));
        const bf16x8 d0 = ldsfrag((lds_cptr)(((unsigned)(uintptr_t)df0 ^ (ks << 5)) + so));
        const bf16x8 d1 = ldsfrag((lds_cptr)(((unsigned)(uintptr_t)df1 ^ (ks << 5)) + so));
        a0 = MFMA(xf, d0, a0);
        a1 = MFMA(xf, d1, a1);
      }
    }
    STAMP(0, 1);
    const int part = cb >> 2;
    const float sg = part ? -1.f : 1.f;
    const int et = (cb & 3) * 4 + 2 * ch, s16 = rb * 8 + rg * 2;
    char* op = ws + WS_AB + ((size_t)((part * 128 + s16) * 16 + et) << 10) + lane * 16;
    *(u32x4*)(op) = natorder(pack8(a0, 0, sg));
    *(u32x4*)(op + 16384) = natorder(pack8(a0, 8, sg));
    *(u32x4*)(op + 1024) = natorder(pack8(a1, 0, sg));
    *(u32x4*)(op + 1024 + 16384) = natorder(pack8(a1, 8, sg));
  }
  STAMP(0, 2);
}

constexpr int K2_KR = 0, K2_VR = 16384, K2_CONST = 32768, K2_C = 36864, K2_B = K2_C + 4 * 16384, K2_LDS = K2_B + 3 * 16384;
#define K2_GEMM 1
constexpr int K2_THREADS = K2_GEMM ? 768 : 512;

__device__ __forceinline__ void attn_exact(const char* ws, int h, int qt, char* optr, int lane) {
  const int hi = lane >> 5;
  const bf16x8 qf = *(const bf16x8*)(ws + WS_Q + ((size_t)(h * 64 + qt) << 10) + lane * 16);
  f32x16 o = {};
  float m = -INFINITY;
  const bool isv = (lane & 31) < 16;
  const short one = ((lane & 31) == 16 || (lane & 31) == 20) ? (short)0x3f80 : (short)0;
  const bf16x8 cst = {one, one, one, one, one, one, one, one};
  for (int t = 0; t < 64; ++t) {
    const bf16x8 kf = *(const bf16x8*)(ws + WS_K + ((size_t)(h * 64 + t) << 10) + lane * 16);
    const char* vp = ws + WS_V + ((size_t)h << 16) + t * 1024 + (hi * 16 + (lane & 15)) * 16;
    bf16x8 v0 = *(const bf16x8*)vp, v1 = *(const bf16x8*)(vp + 512);
    v0 = isv ? v0 : cst; v1 = isv ? v1 : cst;
    f32x16 s = {};
    s = MFMA(kf, qf, s);
    float mx = s[0];
#pragma unroll
    for (int r = 1; r < 16; ++r) mx = fmaxf(mx, s[r]);
    mx = fmaxf(mx, __shfl_xor(mx, 32));
    const float mn = fmaxf(m, mx), al = __builtin_amdgcn_exp2f(m - mn);
    m = mn;
#pragma unroll
    for (int r = 0; r < 16; ++r) { o[r] *= al; s[r] = __builtin_amdgcn_exp2f(s[r] - mn); }
    o = MFMA(v0, __builtin_bit_cast(bf16x8, pack8(s, 0)), o);
    o = MFMA(v1, __builtin_bit_cast(bf16x8, pack8(s, 8)), o);
  }
  *(u32x4*)optr = pack8(o, 0, 1.f / o[8]);
}

__global__ void __launch_bounds__(K2_THREADS, K2_GEMM ? 3 : 2) k2_attn(const char* __restrict__ cre, const char* __restrict__ cim, char* __restrict__ ws) {
  extern __shared__ __attribute__((aligned(16))) char lds[];
  const int lane = threadIdx.x & 63, w = __builtin_amdgcn_readfirstlane(threadIdx.x >> 6), hi = lane >> 5;
  const int x = blockIdx.x & 7, k = blockIdx.x >> 3;
  const unsigned lds0 = (unsigned)(uintptr_t)lds;
  STAMP(1, 0);
  if (w < 8) {
    const int h = 4 * x + (k >> 3), qt = (k & 7) * 8 + w;
    const char* src = ws + (w < 4 ? WS_K : WS_V) + ((size_t)h << 16) + (w & 3) * 1024;
    const unsigned dst = lds0 + (w < 4 ? K2_KR : K2_VR) + (w & 3) * 1024;
    const int ahead = w < 4 ? 3 : 2;
    const unsigned vl = lane * 16;
    for (int i = 0; i < ahead; ++i) dma16(src + i * 4096, vl, dst + i * 4096);
    if (threadIdx.x < 256) {
      const int c = threadIdx.x;
      const unsigned one = ((c & 31) == 0 || (c & 31) == 4) ? 0x3f803f80u : 0u;
      *(LAS u32x4*)((LAS char*)lds + K2_CONST + c * 16) = (u32x4){one, one, one, one};
    }
    const bf16x8 qf = *(const bf16x8*)(ws + WS_Q + ((size_t)(h * 64 + qt) << 10) + lane * 16);
    const int r32 = lane & 31;
    const int cchunk = r32 < 20 ? r32 - 16 : (r32 < 28 ? (r32 == 20 ? 4 : 5) : 1);
    const unsigned vbase = r32 < 16 ? (unsigned)(K2_VR + (hi * 16 + r32) * 16) : (unsigned)(K2_CONST + cchunk * 16);
    const unsigned vstep = r32 < 16 ? 4096u : 0u;
    const lds_cptr kp = (lds_cptr)lds + K2_KR + lane * 16;
    if (w < 4) WAIT_BAR(2); else WAIT_BAR(1);
    f32x16 cm, sA, sB, o = {};
    {
      f32x16 z = {};
      sA = MFMA(ldsfrag(kp), qf, z);
      float mx = sA[0];
#pragma unroll
      for (int r = 1; r < 16; ++r) mx = fmaxf(mx, sA[r]);
      auto sw = __builtin_amdgcn_permlane32_swap(__float_as_uint(mx), __float_as_uint(mx), false, false);
      mx = fmaxf(__uint_as_float(sw[0]), __uint_as_float(sw[1]));
      const float nm = -(mx + 48.f);
#pragma unroll
      for (int r = 0; r < 16; ++r) { cm[r] = nm; sA[r] += nm; }
    }
    PIN(cm);
    u32x4 p0 = {}, p1 = {};
    bf16x8 kf = ldsfrag(kp + 1024), vf0, vf1;
    lds_cptr vp = (lds_cptr)lds + vbase;
    vf0 = ldsfrag(vp);
    vf1 = ldsfrag(vp + 512);
    if (w < 4) WAIT_BAR(1); else WAIT_BAR(0);
#define EX(v) __builtin_amdgcn_exp2f(v)
#define PVM(A, B, C) MFMA(A, B, C)
#define SM(A, B, C, OLD) MFMA(A, B, C)
#define LDF(P, OLD) ldsfrag(P)
#define TILE(CUR, NXT, KNEXT, V0NEXT, V1CUR, LASTT) do { \
      CUR[0] = EX(CUR[0]); CUR[1] = EX(CUR[1]); CUR[2] = EX(CUR[2]); CUR[3] = EX(CUR[3]); \
      p0[0] = cvtpk(CUR[0], CUR[1]); p0[1] = cvtpk(CUR[2], CUR[3]); PIN(p0); SBAR(); \
      o = PVM(vf1, __builtin_bit_cast(bf16x8, p1), o); SBAR(); \
      vf1 = LDF(V1CUR, vf1); SBAR(); \
      CUR[4] = EX(CUR[4]); CUR[5] = EX(CUR[5]); CUR[6] = EX(CUR[6]); CUR[7] = EX(CUR[7]); \
      p0[2] = cvtpk(CUR[4], CUR[5]); p0[3] = cvtpk(CUR[6], CUR[7]); PIN(p0); SBAR(); \
      NXT = SM(kf, qf, cm, NXT); SBAR(); \
      kf = LDF(KNEXT, kf); SBAR(); \
      CUR[8] = EX(CUR[8]); CUR[9] = EX(CUR[9]); CUR[10] = EX(CUR[10]); CUR[11] = EX(CUR[11]); \
      p1[0] = cvtpk(CUR[8], CUR[9]); p1[1] = cvtpk(CUR[10], CUR[11]); PIN(p1); SBAR(); \
      o = PVM(vf0, __builtin_bit_cast(bf16x8, p0), o); SBAR(); \
      if (!(LASTT)) { vf0 = LDF(V0NEXT, vf0); SBAR(); } \
      CUR[12] = EX(CUR[12]); CUR[13] = EX(CUR[13]); CUR[14] = EX(CUR[14]); CUR[15] = EX(CUR[15]); \
      p1[2] = cvtpk(CUR[12], CUR[13]); p1[3] = cvtpk(CUR[14], CUR[15]); PIN(p1); SBAR(); \
    } while (0)
#define STEP(n, sl, sl1, LAST) do { \
      STEPSTAMP(n); \
      if ((n) + ahead < 16) dma16(src + ((n) + ahead) * 4096, vl, dst + (((n) + ahead) & 3) * 4096); \
      const lds_cptr k0 = kp + (sl) * 4096, k1 = kp + (sl1) * 4096; \
      const lds_cptr v0 = (lds_cptr)lds + vbase + (sl) * vstep; \
      const lds_cptr v1 = (lds_cptr)lds + vbase + (sl1) * vstep; \
      TILE(sA, sB, k0 + 2048, v0 + 1024, v0 + 512, 0); \
      TILE(sB, sA, k0 + 3072, v0 + 2048, v0 + 1536, 0); \
      TILE(sA, sB, k1, v0 + 3072, v0 + 2560, 0); \
        \
      TILE(sB, sA, k1 + 1024, v0 + 3072, v0 + 3584, 1); \
      if ((n) < 13) WAIT_BAR(1); else WAIT_BAR(0); \
      vf0 = LDF(v1, vf0); \
    } while (0)
    STAMP(1, 3); STAMPC(1, 8);
    for (int n = 0; n < 16; n += 4) {
      STEP(n, 0, 1, 0); STEP(n + 1, 1, 2, 0); STEP(n + 2, 2, 3, 0); STEP(n + 3, 3, 0, 0);
    }
    o = MFMA(vf1, __builtin_bit_cast(bf16x8, p1), o);
    STAMP(1, 1); STAMPC(1, 9); STEPSTAMP(16);
    char* optr = ws + WS_O + ((size_t)(qt * 32 + h) << 10) + lane * 16;
    const float l = o[8];
    const bool bad = !(l > 0x1p-100f && l < 0x1p100f);
    if (__builtin_expect(__any(bad), 0)) attn_exact(ws, h, qt, optr, lane);
    else *(u32x4*)optr = pack8(o, 0, 1.f / l);
  }
  else {
    const int g = w - 8;
    const int ksp = x >> 1, tb = (x & 1) * 8 + (k >> 2), eb = k & 3;
#define GPRIO 2
#define GSLEEP 0
    __builtin_amdgcn_s_setprio(GPRIO);
    const char* src;
    unsigned voff[2];
    unsigned dst;
    size_t sstep;
    int ahead, nslot;
    if (g < 2) {
      src = ((ksp >> 1) ? cim : cre) + (size_t)(tb * 128 + g * 64) * 4096 + (ksp & 1) * 2048;
#pragma unroll
      for (int j = 0; j < 2; ++j) { const int row = 8 * j + (lane >> 3); voff[j] = row * 4096 + (((lane & 7) ^ ((row >> 1) & 7)) << 4); }
      dst = lds0 + K2_C + g * 8192; sstep = 128; ahead = 3; nslot = 4;
    } else {
      src = ws + WS_AB + ((size_t)(((ksp >> 1) * 128 + (ksp & 1) * 64 + 2 * (g - 2)) * 16 + eb * 4) << 10);
      voff[0] = lane * 16; voff[1] = lane * 16;
      dst = lds0 + K2_B + (g - 2) * 8192; sstep = 4 * 16 * 1024; ahead = 2; nslot = 3;
    }
    auto issue = [&](int n, int slot) {
      const char* p = src + (size_t)n * sstep;
      const unsigned d = dst + slot * 16384;
      if (g < 2) {
#pragma unroll
        for (int i = 0; i < 8; ++i) dma16(p + (size_t)(i >> 1) * 16 * 4096, voff[i & 1], d + i * 1024);
      } else {
#pragma unroll
        for (int i = 0; i < 8; ++i) dma16(p + (i >> 2) * 16384 + (i & 3) * 1024, voff[0], d + i * 1024);
      }
    };
    for (int i = 0; i < ahead; ++i) issue(i, i);
    const int tm = g & 1, en = g >> 1;
    const int tr0 = 64 * tm + (lane & 31), tr1 = tr0 + 32;
    const unsigned cf0 = K2_C + tr0 * 128 + ((hi ^ ((tr0 >> 1) & 7)) << 4), cf1 = K2_C + tr1 * 128 + ((hi ^ ((tr1 >> 1) & 7)) << 4);
    const unsigned bf = K2_B + en * 2048 + lane * 16;
    f32x16 a00 = {}, a01 = {}, a10 = {}, a11 = {};
    if (g < 2) WAIT_BAR(16); else WAIT_BAR(8);
    asm volatile("s_barrier" ::: "memory");
    int cs = 0, bs = 0;
    for (int n = 0; n < 16; ++n) {
      if (n + ahead < 16) issue(n + ahead, g < 2 ? ((cs + 3) & 3) : (bs == 0 ? 2 : bs - 1));
      const lds_cptr cb_ = (lds_cptr)lds + cs * 16384, bb_ = (lds_cptr)lds + bf + bs * 16384;
#pragma unroll
      for (int ks = 0; ks < 4; ++ks) {
        const bf16x8 c0 = ldsfrag(cb_ + (cf0 ^ (ks << 5))), c1 = ldsfrag(cb_ + (cf1 ^ (ks << 5)));
        const bf16x8 b0 = ldsfrag(bb_ + ks * 4096), b1 = ldsfrag(bb_ + ks * 4096 + 1024);
        a00 = MFMA(b0, c0, a00); a01 = MFMA(b0, c1, a01);
        a10 = MFMA(b1, c0, a10); a11 = MFMA(b1, c1, a11);
      }
      if (g < 2) { if (n < 13) WAIT_BAR(16); else if (n == 13) WAIT_BAR(8); else WAIT_BAR(0); }
      else { if (n < 14) WAIT_BAR(8); else WAIT_BAR(0); }
      cs = (cs + 1) & 3; bs = bs == 2 ? 0 : bs + 1;
    }
    const int tt = tb * 4 + 2 * tm, et = eb * 4 + 2 * en;
    char* pp = ws + WS_P + ((size_t)((ksp * 64 + tt) * 16 + et) << 11) + lane * 16;
    *(u32x4*)(pp) = pack8(a00, 0); *(u32x4*)(pp + 1024) = pack8(a00, 8);
    *(u32x4*)(pp + 2048) = pack8(a10, 0); *(u32x4*)(pp + 2048 + 1024) = pack8(a10, 8);
    *(u32x4*)(pp + 32768) = pack8(a01, 0); *(u32x4*)(pp + 32768 + 1024) = pack8(a01, 8);
    *(u32x4*)(pp + 32768 + 2048) = pack8(a11, 0); *(u32x4*)(pp + 32768 + 2048 + 1024) = pack8(a11, 8);
    STAMP(1, 1);
  }
  STAMP(1, 2);
}

constexpr int K3_LDS = 65536;
__global__ void __launch_bounds__(512, 2) k3_out(const char* __restrict__ wo, const char* __restrict__ ws_, char* __restrict__ out) {
  extern __shared__ __attribute__((aligned(16))) char lds[];
  char* ws = (char*)ws_;
  const int lane = threadIdx.x & 63, w = __builtin_amdgcn_readfirstlane(threadIdx.x >> 6), hi = lane >> 5;
  const int x = blockIdx.x & 7, k = blockIdx.x >> 3;
  const unsigned lds0 = (unsigned)(uintptr_t)lds;
  STAMP(2, 0);
  const int tb = 4 * x + (k >> 3), nb = k & 7;
  {
    const int r = lane >> 3;
    const unsigned vo = r * 1024 + (((lane & 7) ^ (((r >> 1) & 1) << 2)) << 4);
#pragma unroll
    for (int i = 0; i < 8; ++i) dma16(wo + nb * 128 + (size_t)(w * 8 + i) * 8 * 1024, vo, lds0 + (w * 8 + i) * 1024);
  }
  const int kh = w >> 2, tm = w & 1, nn = (w >> 1) & 1;
  const int tt = tb * 2 + tm;
  bf16x8 of[16];
#pragma unroll
  for (int i = 0; i < 16; ++i) of[i] = *(const bf16x8*)(ws + WS_O + ((size_t)(tt * 32 + kh * 16 + i) << 10) + lane * 16);
  f32x16 ys;
  {
    const int et = nb * 2 + nn;
    const char* pp = ws + WS_P + ((size_t)((2 * kh * 64 + tt) * 16 + et) << 11) + lane * 16;
    const u32x4 a0 = *(const u32x4*)pp, a1 = *(const u32x4*)(pp + 1024);
    const u32x4 b0 = *(const u32x4*)(pp + (64 * 16 << 11)), b1 = *(const u32x4*)(pp + (64 * 16 << 11) + 1024);
#pragma unroll
    for (int i = 0; i < 4; ++i) {
      ys[2 * i] = __uint_as_float(a0[i] << 16) + __uint_as_float(b0[i] << 16);
      ys[2 * i + 1] = __uint_as_float(a0[i] & 0xffff0000u) + __uint_as_float(b0[i] & 0xffff0000u);
      ys[8 + 2 * i] = __uint_as_float(a1[i] << 16) + __uint_as_float(b1[i] << 16);
      ys[8 + 2 * i + 1] = __uint_as_float(a1[i] & 0xffff0000u) + __uint_as_float(b1[i] & 0xffff0000u);
    }
  }
  asm volatile("s_waitcnt vmcnt(0)\n\ts_barrier" ::: "memory");
  const int q4 = (lane & 15) >> 2, p4 = lane & 3, blk = (lane >> 4) & 1;
  const lds_cptr wf = (lds_cptr)lds + (kh * 256 + 4 * hi + q4) * 128 + (((4 * nn + 2 * blk + (p4 >> 1)) ^ ((q4 >> 1) << 2)) << 4) + (p4 & 1) * 8;
  f32x16 acc = {};
#pragma unroll
  for (int i = 0; i < 16; ++i) {
    const bf16x8 a = cat8(ldstr(wf + i * 2048), ldstr(wf + i * 2048 + 1024));
    acc = MFMA(a, of[i], acc);
  }
  __syncthreads();
  if (kh == 1) {
    LAS float* r = (LAS float*)((LAS char*)lds + (w & 3) * 8192);
#pragma unroll
    for (int i = 0; i < 4; ++i) {
      *(LAS f32x4*)(r + i * 256 + lane * 4) = (f32x4){acc[4 * i], acc[4 * i + 1], acc[4 * i + 2], acc[4 * i + 3]};
      *(LAS f32x4*)(r + 1024 + i * 256 + lane * 4) = (f32x4){ys[4 * i], ys[4 * i + 1], ys[4 * i + 2], ys[4 * i + 3]};
    }
  }
  __syncthreads();
  if (kh == 0) {
    const LAS float* r = (const LAS float*)((LAS char*)lds + (w & 3) * 8192);
#pragma unroll
    for (int i = 0; i < 4; ++i) {
      const f32x4 u = *(const LAS f32x4*)(r + i * 256 + lane * 4);
      const f32x4 v = *(const LAS f32x4*)(r + 1024 + i * 256 + lane * 4);
#pragma unroll
      for (int j = 0; j < 4; ++j) { acc[4 * i + j] += u[j]; ys[4 * i + j] += v[j]; }
    }
    const int row = tt * 32 + (lane & 31);
    char* op = out + (size_t)row * 2048 + (nb * 64 + nn * 32 + hi * 8) * 2;
    *(u32x4*)(op) = natorder(pack8(acc, 0));
    *(u32x4*)(op + 32) = natorder(pack8(acc, 8));
    *(u32x4*)(op + 1024) = natorder(pack8(ys, 0));
    *(u32x4*)(op + 1024 + 32) = natorder(pack8(ys, 8));
  }
  STAMP(2, 2);
}

extern "C" void kernel_launch(void* const* d_in, const int* in_sizes, int n_in, void* d_out, int out_size, void* d_ws, size_t ws_size, hipStream_t stream) {
  const char* xa = (const char*)d_in[0];
  const char* wqkv = (const char*)d_in[1];
  const char* wo = (const char*)d_in[2];
  const char* xb = (const char*)d_in[3];
  const char* cre = (const char*)d_in[4];
  const char* cim = (const char*)d_in[5];
  const char* dre = (const char*)d_in[6];
  const char* dim = (const char*)d_in[7];
  char* ws = (char*)d_ws;
  hipFuncSetAttribute((const void*)k1_proj, hipFuncAttributeMaxDynamicSharedMemorySize, K1_LDS);
  hipFuncSetAttribute((const void*)k2_attn, hipFuncAttributeMaxDynamicSharedMemorySize, K2_LDS);
  hipFuncSetAttribute((const void*)k3_out, hipFuncAttributeMaxDynamicSharedMemorySize, K3_LDS);
  k1_proj<<<256, 512, K1_LDS, stream>>>(xa, wqkv, xb, dre, dim, ws);
  k2_attn<<<256, K2_THREADS, K2_LDS, stream>>>(cre, cim, ws);
  k3_out<<<256, 512, K3_LDS, stream>>>(wo, ws, (char*)d_out);
}
```

```cpp
#include <hip/hip_runtime.h>
#include <hip/hip_bf16.h>
#include <stdint.h>

typedef __attribute__((ext_vector_type(8))) short bf16x8;
typedef __attribute__((ext_vector_type(4))) short s16x4;
typedef __attribute__((ext_vector_type(16))) float f32x16;
typedef __attribute__((ext_vector_type(4))) unsigned u32x4;
typedef __attribute__((ext_vector_type(2))) unsigned u32x2;
typedef __attribute__((ext_vector_type(4))) float f32x4;
#define LAS __attribute__((address_space(3)))
typedef LAS const char* lds_cptr;

#define SBAR() __builtin_amdgcn_sched_barrier(0)
#define PIN(x) asm volatile("" : "+v"(x))
#define MFMA(a, b, c) __builtin_amdgcn_mfma_f32_32x32x16_bf16(a, b, c, 0, 0, 0)

constexpr int S = 2048, DM = 512, NH = 32;
constexpr size_t WS_Q = 0;
constexpr size_t WS_K = 2u << 20;
constexpr size_t WS_V = 4u << 20;
constexpr size_t WS_AB = 6u << 20;
constexpr size_t WS_O = 10u << 20;
constexpr size_t WS_P = 12u << 20;
#define STEPSTAMP(n)
#define STAMP(kid, i)
#define STAMPC(kid, i)

typedef __attribute__((ext_vector_type(2))) float f32x2;
typedef __attribute__((ext_vector_type(2))) __bf16 bf16x2;
__device__ __forceinline__ unsigned cvtpk(float lo, float hi) { const f32x2 v = {lo, hi}; return __builtin_bit_cast(unsigned, __builtin_convertvector(v, bf16x2)); }
__device__ __forceinline__ void dma16(const void* sbase, unsigned voff, unsigned ldsaddr) {
  asm volatile("s_mov_b32 m0, %2\n\ts_nop 0\n\tglobal_load_lds_dwordx4 %0, %1" ::"v"(voff), "s"(sbase), "s"(ldsaddr) : "memory");
}
#define WAIT_BAR(N) asm volatile("s_waitcnt vmcnt(" #N ") lgkmcnt(0)\n\ts_barrier" ::: "memory")
__device__ __forceinline__ bf16x8 ldsfrag(lds_cptr p) { return *(LAS const bf16x8*)p; }
typedef short v4i16_t __attribute__((ext_vector_type(4)));
__device__ __forceinline__ s16x4 ldstr(lds_cptr p) { return __builtin_bit_cast(s16x4, __builtin_amdgcn_ds_read_tr16_b64_v4i16((LAS v4i16_t*)p)); }
__device__ __forceinline__ bf16x8 cat8(s16x4 a, s16x4 b) { return (bf16x8){a[0], a[1], a[2], a[3], b[0], b[1], b[2], b[3]}; }
__device__ __forceinline__ u32x4 pack8(const f32x16& c, int r0, float sc) {
  return (u32x4){cvtpk(c[r0] * sc, c[r0 + 1] * sc), cvtpk(c[r0 + 2] * sc, c[r0 + 3] * sc), cvtpk(c[r0 + 4] * sc, c[r0 + 5] * sc), cvtpk(c[r0 + 6] * sc, c[r0 + 7] * sc)};
}
__device__ __forceinline__ u32x4 pack8(const f32x16& c, int r0) {
  return (u32x4){cvtpk(c[r0], c[r0 + 1]), cvtpk(c[r0 + 2], c[r0 + 3]), cvtpk(c[r0 + 4], c[r0 + 5]), cvtpk(c[r0 + 6], c[r0 + 7])};
}
__device__ __forceinline__ u32x4 natorder(u32x4 w) {
  auto a = __builtin_amdgcn_permlane32_swap(w[0], w[2], false, false);
  auto b = __builtin_amdgcn_permlane32_swap(w[1], w[3], false, false);
  return (u32x4){a[0], b[0], a[1], b[1]};
}

constexpr int K1_SLOT = 40960, K1_LDS = 4 * K1_SLOT;
#define RA 2
#define ST_AUX 17
__device__ __forceinline__ void st16(__amdgpu_buffer_rsrc_t r, unsigned off, u32x4 v) { __builtin_amdgcn_raw_buffer_store_b128(v, r, off, 0, ST_AUX); }
__device__ __forceinline__ __amdgpu_buffer_rsrc_t mkrsrc(const void* p, unsigned bytes) { return __builtin_amdgcn_make_buffer_rsrc((void*)p, (short)0, (int)bytes, 0x00020000); }
#define WAITV(N) asm volatile("s_waitcnt vmcnt(" #N ")" ::: "memory")
#define WAITL() asm volatile("s_waitcnt lgkmcnt(0)" ::: "memory")
#define BAR() asm volatile("s_barrier" ::: "memory")

#define dma16k1 dma16
__global__ void __launch_bounds__(512, 2) k1_proj(const char* __restrict__ xa, const char* __restrict__ wqkv, const char* __restrict__ xb,
                                                  const char* __restrict__ dre, const char* __restrict__ dim, char* __restrict__ ws) {
  extern __shared__ __attribute__((aligned(16))) char lds[];
  const int lane = threadIdx.x & 63, w = __builtin_amdgcn_readfirstlane(threadIdx.x >> 6), hi = lane >> 5;
  const int x = blockIdx.x & 7, k = blockIdx.x >> 3;
  const int rb = (x & 3) * 4 + (k & 3), cb = (x >> 2) * 4 + ((k >> 2) & 3);
  const unsigned lds0 = (unsigned)(uintptr_t)lds;
  const bool qkv = k < 16;
  STAMP(0, 0);
  if (w >= 4) {
    const int p = w - 4;
    const int rot = (k + x) & 7;
    unsigned vx[2];
#pragma unroll
    for (int j = 0; j < 2; ++j) { const int row = 8 * j + (lane >> 3); vx[j] = row * 1024 + (((lane & 7) ^ ((row >> 1) & 7)) << 4); }
    if (qkv) {
      const char* xs = xa + (size_t)(rb * 128 + p * 32) * 1024;
      const char* wsrc = wqkv + cb * 128 + (size_t)p * 16 * 3072;
      const int wr = lane >> 3;
      const unsigned vw = wr * 3072 + (((lane & 7) ^ (((wr >> 1) & 1) << 2)) << 4);
      auto issue = [&](int s) {
        const int ks = (s + rot) & 7;
        const unsigned sl = lds0 + (s & 3) * K1_SLOT;
        const char* xp = xs + ks * 128;
        const char* wp = wsrc + (size_t)ks * 64 * 3072;
#pragma unroll
        for (int i = 0; i < 4; ++i) dma16k1(xp + (i >> 1) * 16384, vx[i & 1], sl + p * 4096 + i * 1024);
#pragma unroll
        for (int i = 0; i < 6; ++i) dma16k1(wp + (i >> 1) * 1024 + (i & 1) * 8 * 3072, vw, sl + 16384 + (i >> 1) * 8192 + p * 2048 + (i & 1) * 1024);
      };
      issue(0); issue(1);
      WAITV(10); BAR();
      issue(2);
      for (int s = 1; s < 8; ++s) {
        if (s < 7) WAITV(10); else WAITV(0);
        BAR();
        if (s + RA < 8) issue(s + RA);
      }
    } else {
      const char* xs = xb + (size_t)(rb * 128 + p * 32) * 1024;
      const char* ds = ((cb >> 2) ? dim : dre) + (size_t)((cb & 3) * 128 + p * 32) * 1024;
      auto issue = [&](int s) {
        const int ks = (s + rot) & 7;
        const unsigned sl = lds0 + (s & 3) * K1_SLOT;
#pragma unroll
        for (int i = 0; i < 4; ++i) dma16k1(xs + ks * 128 + (i >> 1) * 16384, vx[i & 1], sl + p * 4096 + i * 1024);
#pragma unroll
        for (int i = 0; i < 4; ++i) dma16k1(ds + ks * 128 + (i >> 1) * 16384, vx[i & 1], sl + 16384 + p * 4096 + i * 1024);
      };
      issue(0); issue(1);
      WAITV(8); BAR();
      issue(2);
      for (int s = 1; s < 8; ++s) {
        if (s < 7) WAITV(8); else WAITV(0);
        BAR();
        if (s + RA < 8) issue(s + RA);
      }
    }
    return;
  }
  const __amdgpu_buffer_rsrc_t wsr = mkrsrc(ws, WS_P);
  const int tm = w & 1, ch = w >> 1;
  const int frow = 64 * tm + (lane & 31);
  const unsigned xa0 = lds0 + frow * 128 + ((hi ^ ((frow >> 1) & 7)) << 4);
  auto ldx = [&](unsigned so, int ks, int t) { return ldsfrag((lds_cptr)(uintptr_t)((xa0 ^ (ks << 5)) + so) + t * 4096); };
  if (qkv) {
    const int q4 = (lane & 15) >> 2, p4 = lane & 3, blk = (lane >> 4) & 1;
    const unsigned wa0 = lds0 + 16384 + (8 * hi + q4) * 128 + (((4 * ch + 2 * blk + (p4 >> 1)) ^ ((q4 >> 1) << 2)) << 4) + (p4 & 1) * 8;
    auto ldw = [&](unsigned so, int ks, int m) { const lds_cptr wp = (lds_cptr)(uintptr_t)(wa0 + so) + ks * 2048 + m * 8192; return cat8(ldstr(wp), ldstr(wp + 512)); };
    f32x16 aq0 = {}, aq1 = {}, ak0 = {}, ak1 = {}, av0 = {}, av1 = {};
    bf16x8 x0, x1, wq, wk, wv, nx0, nx1, nwq, nwk, nwv;
    BAR();
    STAMP(0, 8); STAMPC(0, 24);
    x0 = ldx(0, 0, 0); wq = ldw(0, 0, 0); x1 = ldx(0, 0, 1); wk = ldw(0, 0, 1); wv = ldw(0, 0, 2);
#define QSUB(so, ks, SYNC, LOAD) do { \
      if (SYNC) WAITL(); SBAR(); \
      aq0 = MFMA(wq, x0, aq0); SBAR(); \
      if (SYNC) BAR(); SBAR(); \
      if (LOAD) { nx0 = ldx(so, ks, 0); nwq = ldw(so, ks, 0); } SBAR(); \
      aq1 = MFMA(wq, x1, aq1); SBAR(); \
      if (LOAD) { nx1 = ldx(so, ks, 1); nwk = ldw(so, ks, 1); } SBAR(); \
      ak0 = MFMA(wk, x0, ak0); SBAR(); \
      if (LOAD) { nwv = ldw(so, ks, 2); } SBAR(); \
      ak1 = MFMA(wk, x1, ak1); SBAR(); \
      av0 = MFMA(x0, wv, av0); SBAR(); \
      av1 = MFMA(x1, wv, av1); SBAR(); \
      if (LOAD) { x0 = nx0; x1 = nx1; wq = nwq; wk = nwk; wv = nwv; } \
    } while (0)
#pragma unroll
    for (int s = 0; s < 8; ++s) {
      const unsigned so = (s & 3) * K1_SLOT, sn = ((s + 1) & 3) * K1_SLOT;
      QSUB(so, 1, false, true); QSUB(so, 2, false, true); QSUB(so, 3, false, true);
      QSUB(sn, 0, s < 7, s < 7);
    }
    STAMP(0, 1); STAMPC(0, 25);
    constexpr float QS = 0.25f * 1.4426950408889634f;
    const int h0 = 4 * cb + 2 * ch, tile = rb * 4 + 2 * tm;
    const unsigned qo = WS_Q + ((h0 * 64 + tile) << 10) + lane * 16;
    st16(wsr, qo, pack8(aq0, 0, QS)); st16(wsr, qo + 65536, pack8(aq0, 8, QS));
    st16(wsr, qo + 1024, pack8(aq1, 0, QS)); st16(wsr, qo + 1024 + 65536, pack8(aq1, 8, QS));
    const unsigned ko = qo + (unsigned)(WS_K - WS_Q);
    st16(wsr, ko, pack8(ak0, 0)); st16(wsr, ko + 65536, pack8(ak0, 8));
    st16(wsr, ko + 1024, pack8(ak1, 0)); st16(wsr, ko + 1024 + 65536, pack8(ak1, 8));
    const unsigned vo = WS_V + ((h0 + ((lane >> 4) & 1)) << 16) + tile * 1024 + (hi * 16 + (lane & 15)) * 16;
    st16(wsr, vo, pack8(av0, 0)); st16(wsr, vo + 512, pack8(av0, 8));
    st16(wsr, vo + 1024, pack8(av1, 0)); st16(wsr, vo + 1024 + 512, pack8(av1, 8));
  } else {
    const int er = 64 * ch + (lane & 31);
    const unsigned da0 = lds0 + 16384 + er * 128 + ((hi ^ ((er >> 1) & 7)) << 4);
    auto ldd = [&](unsigned so, int ks, int t) { return ldsfrag((lds_cptr)(uintptr_t)((da0 ^ (ks << 5)) + so) + t * 4096); };
    f32x16 a00 = {}, a01 = {}, a10 = {}, a11 = {};
    bf16x8 x0, x1, d0, d1, nx0, nx1, nd0, nd1;
    BAR();
    STAMP(0, 17);
    x0 = ldx(0, 0, 0); d0 = ldd(0, 0, 0); d1 = ldd(0, 0, 1); x1 = ldx(0, 0, 1);
#define DSUB(so, ks, SYNC, LOAD) do { \
      if (SYNC) WAITL(); SBAR(); \
      a00 = MFMA(x0, d0, a00); SBAR(); \
      if (SYNC) BAR(); SBAR(); \
      if (LOAD) { nx0 = ldx(so, ks, 0); nd0 = ldd(so, ks, 0); } SBAR(); \
      a01 = MFMA(x0, d1, a01); SBAR(); \
      if (LOAD) { nd1 = ldd(so, ks, 1); nx1 = ldx(so, ks, 1); } SBAR(); \
      a10 = MFMA(x1, d0, a10); SBAR(); \
      a11 = MFMA(x1, d1, a11); SBAR(); \
      if (LOAD) { x0 = nx0; x1 = nx1; d0 = nd0; d1 = nd1; } \
    } while (0)
#pragma unroll
    for (int s = 0; s < 8; ++s) {
      const unsigned so = (s & 3) * K1_SLOT, sn = ((s + 1) & 3) * K1_SLOT;
      DSUB(so, 1, false, true); DSUB(so, 2, false, true); DSUB(so, 3, false, true);
      DSUB(sn, 0, s < 7, s < 7);
    }
    STAMP(0, 3);
    const int part = cb >> 2;
    const float sg = part ? -1.f : 1.f;
    const int et = (cb & 3) * 4 + 2 * ch, s16 = rb * 8 + tm * 4;
    const unsigned op = WS_AB + (((part * 128 + s16) * 16 + et) << 10) + lane * 16;
    st16(wsr, op, natorder(pack8(a00, 0, sg))); st16(wsr, op + 16384, natorder(pack8(a00, 8, sg)));
    st16(wsr, op + 1024, natorder(pack8(a01, 0, sg))); st16(wsr, op + 1024 + 16384, natorder(pack8(a01, 8, sg)));
    st16(wsr, op + 32768, natorder(pack8(a10, 0, sg))); st16(wsr, op + 32768 + 16384, natorder(pack8(a10, 8, sg)));
    st16(wsr, op + 32768 + 1024, natorder(pack8(a11, 0, sg))); st16(wsr, op + 32768 + 1024 + 16384, natorder(pack8(a11, 8, sg)));
  }
  STAMP(0, 2);
}

constexpr int K2_KR = 0, K2_VR = 16384, K2_CONST = 32768, K2_C = 36864, K2_B = K2_C + 4 * 16384, K2_LDS = K2_B + 3 * 16384;
#define K2_GEMM 1
constexpr int K2_THREADS = K2_GEMM ? 768 : 512;

__device__ __forceinline__ void attn_exact(const char* ws, int h, int qt, char* optr, int lane) {
  const int hi = lane >> 5;
  const bf16x8 qf = *(const bf16x8*)(ws + WS_Q + ((size_t)(h * 64 + qt) << 10) + lane * 16);
  f32x16 o = {};
  float m = -INFINITY;
  const bool isv = (lane & 31) < 16;
  const short one = ((lane & 31) == 16 || (lane & 31) == 20) ? (short)0x3f80 : (short)0;
  const bf16x8 cst = {one, one, one, one, one, one, one, one};
  for (int t = 0; t < 64; ++t) {
    const bf16x8 kf = *(const bf16x8*)(ws + WS_K + ((size_t)(h * 64 + t) << 10) + lane * 16);
    const char* vp = ws + WS_V + ((size_t)h << 16) + t * 1024 + (hi * 16 + (lane & 15)) * 16;
    bf16x8 v0 = *(const bf16x8*)vp, v1 = *(const bf16x8*)(vp + 512);
    v0 = isv ? v0 : cst; v1 = isv ? v1 : cst;
    f32x16 s = {};
    s = MFMA(kf, qf, s);
    float mx = s[0];
#pragma unroll
    for (int r = 1; r < 16; ++r) mx = fmaxf(mx, s[r]);
    mx = fmaxf(mx, __shfl_xor(mx, 32));
    const float mn = fmaxf(m, mx), al = __builtin_amdgcn_exp2f(m - mn);
    m = mn;
#pragma unroll
    for (int r = 0; r < 16; ++r) { o[r] *= al; s[r] = __builtin_amdgcn_exp2f(s[r] - mn); }
    o = MFMA(v0, __builtin_bit_cast(bf16x8, pack8(s, 0)), o);
    o = MFMA(v1, __builtin_bit_cast(bf16x8, pack8(s, 8)), o);
  }
  *(u32x4*)optr = pack8(o, 0, 1.f / o[8]);
}

__global__ void __launch_bounds__(K2_THREADS, K2_GEMM ? 3 : 2) k2_attn(const char* __restrict__ cre, const char* __restrict__ cim, char* __restrict__ ws) {
  extern __shared__ __attribute__((aligned(16))) char lds[];
  const int lane = threadIdx.x & 63, w = __builtin_amdgcn_readfirstlane(threadIdx.x >> 6), hi = lane >> 5;
  const int x = blockIdx.x & 7, k = blockIdx.x >> 3;
  const unsigned lds0 = (unsigned)(uintptr_t)lds;
  STAMP(1, 0);
  if (w < 8) {
    const int h = 4 * x + (k >> 3), qt = (k & 7) * 8 + w;
    const char* src = ws + (w < 4 ? WS_K : WS_V) + ((size_t)h << 16) + (w & 3) * 1024;
    const unsigned dst = lds0 + (w < 4 ? K2_KR : K2_VR) + (w & 3) * 1024;
    const int ahead = w < 4 ? 3 : 2;
    const unsigned vl = lane * 16;
    for (int i = 0; i < ahead; ++i) dma16(src + i * 4096, vl, dst + i * 4096);
    if (threadIdx.x < 256) {
      const int c = threadIdx.x;
      const unsigned one = ((c & 31) == 0 || (c & 31) == 4) ? 0x3f803f80u : 0u;
      *(LAS u32x4*)((LAS char*)lds + K2_CONST + c * 16) = (u32x4){one, one, one, one};
    }
    const bf16x8 qf = *(const bf16x8*)(ws + WS_Q + ((size_t)(h * 64 + qt) << 10) + lane * 16);
    const int r32 = lane & 31;
    const int cchunk = r32 < 20 ? r32 - 16 : (r32 < 28 ? (r32 == 20 ? 4 : 5) : 1);
    const unsigned vbase = r32 < 16 ? (unsigned)(K2_VR + (hi * 16 + r32) * 16) : (unsigned)(K2_CONST + cchunk * 16);
    const unsigned vstep = r32 < 16 ? 4096u : 0u;
    const lds_cptr kp = (lds_cptr)lds + K2_KR + lane * 16;
    if (w < 4) WAIT_BAR(2); else WAIT_BAR(1);
    f32x16 cm, sA, sB, o = {};
    {
      f32x16 z = {};
      sA = MFMA(ldsfrag(kp), qf, z);
      float mx = sA[0];
#pragma unroll
      for (int r = 1; r < 16; ++r) mx = fmaxf(mx, sA[r]);
      auto sw = __builtin_amdgcn_permlane32_swap(__float_as_uint(mx), __float_as_uint(mx), false, false);
      mx = fmaxf(__uint_as_float(sw[0]), __uint_as_float(sw[1]));
      const float nm = -(mx + 48.f);
#pragma unroll
      for (int r = 0; r < 16; ++r) { cm[r] = nm; sA[r] += nm; }
    }
    PIN(cm);
    u32x4 p0 = {}, p1 = {};
    bf16x8 kf = ldsfrag(kp + 1024), vf0, vf1;
    lds_cptr vp = (lds_cptr)lds + vbase;
    vf0 = ldsfrag(vp);
    vf1 = ldsfrag(vp + 512);
    if (w < 4) WAIT_BAR(1); else WAIT_BAR(0);
#define EX(v) __builtin_amdgcn_exp2f(v)
#define PVM(A, B, C) MFMA(A, B, C)
#define SM(A, B, C, OLD) MFMA(A, B, C)
#define LDF(P, OLD) ldsfrag(P)
#define TILE(CUR, NXT, KNEXT, V0NEXT, V1CUR, LASTT) do { \
      CUR[0] = EX(CUR[0]); CUR[1] = EX(CUR[1]); CUR[2] = EX(CUR[2]); CUR[3] = EX(CUR[3]); \
      p0[0] = cvtpk(CUR[0], CUR[1]); p0[1] = cvtpk(CUR[2], CUR[3]); PIN(p0); SBAR(); \
      o = PVM(vf1, __builtin_bit_cast(bf16x8, p1), o); SBAR(); \
      vf1 = LDF(V1CUR, vf1); SBAR(); \
      CUR[4] = EX(CUR[4]); CUR[5] = EX(CUR[5]); CUR[6] = EX(CUR[6]); CUR[7] = EX(CUR[7]); \
      p0[2] = cvtpk(CUR[4], CUR[5]); p0[3] = cvtpk(CUR[6], CUR[7]); PIN(p0); SBAR(); \
      NXT = SM(kf, qf, cm, NXT); SBAR(); \
      kf = LDF(KNEXT, kf); SBAR(); \
      CUR[8] = EX(CUR[8]); CUR[9] = EX(CUR[9]); CUR[10] = EX(CUR[10]); CUR[11] = EX(CUR[11]); \
      p1[0] = cvtpk(CUR[8], CUR[9]); p1[1] = cvtpk(CUR[10], CUR[11]); PIN(p1); SBAR(); \
      o = PVM(vf0, __builtin_bit_cast(bf16x8, p0), o); SBAR(); \
      if (!(LASTT)) { vf0 = LDF(V0NEXT, vf0); SBAR(); } \
      CUR[12] = EX(CUR[12]); CUR[13] = EX(CUR[13]); CUR[14] = EX(CUR[14]); CUR[15] = EX(CUR[15]); \
      p1[2] = cvtpk(CUR[12], CUR[13]); p1[3] = cvtpk(CUR[14], CUR[15]); PIN(p1); SBAR(); \
    } while (0)
#define STEP(n, sl, sl1, LAST) do { \
      STEPSTAMP(n); \
      if ((n) + ahead < 16) dma16(src + ((n) + ahead) * 4096, vl, dst + (((n) + ahead) & 3) * 4096); \
      const lds_cptr k0 = kp + (sl) * 4096, k1 = kp + (sl1) * 4096; \
      const lds_cptr v0 = (lds_cptr)lds + vbase + (sl) * vstep; \
      const lds_cptr v1 = (lds_cptr)lds + vbase + (sl1) * vstep; \
      TILE(sA, sB, k0 + 2048, v0 + 1024, v0 + 512, 0); \
      TILE(sB, sA, k0 + 3072, v0 + 2048, v0 + 1536, 0); \
      TILE(sA, sB, k1, v0 + 3072, v0 + 2560, 0); \
        \
      TILE(sB, sA, k1 + 1024, v0 + 3072, v0 + 3584, 1); \
      if ((n) < 13) WAIT_BAR(1); else WAIT_BAR(0); \
      vf0 = LDF(v1, vf0); \
    } while (0)
    STAMP(1, 3); STAMPC(1, 24);
    for (int n = 0; n < 16; n += 4) {
      STEP(n, 0, 1, 0); STEP(n + 1, 1, 2, 0); STEP(n + 2, 2, 3, 0); STEP(n + 3, 3, 0, 0);
    }
    o = MFMA(vf1, __builtin_bit_cast(bf16x8, p1), o);
    STAMP(1, 1); STAMPC(1, 25); STEPSTAMP(16);
    char* optr = ws + WS_O + ((size_t)(qt * 32 + h) << 10) + lane * 16;
    const float l = o[8];
    const bool bad = !(l > 0x1p-100f && l < 0x1p100f);
    if (__builtin_expect(__any(bad), 0)) attn_exact(ws, h, qt, optr, lane);
    else *(u32x4*)optr = pack8(o, 0, 1.f / l);
  }
  else {
    const int g = w - 8;
    const int ksp = x >> 1, tb = (x & 1) * 8 + (k >> 2), eb = k & 3;
#define GPRIO 2
#define GSLEEP 0
    __builtin_amdgcn_s_setprio(GPRIO);
    const char* src;
    unsigned voff[2];
    unsigned dst;
    size_t sstep;
    int ahead, nslot;
    if (g < 2) {
      src = ((ksp >> 1) ? cim : cre) + (size_t)(tb * 128 + g * 64) * 4096 + (ksp & 1) * 2048;
#pragma unroll
      for (int j = 0; j < 2; ++j) { const int row = 8 * j + (lane >> 3); voff[j] = row * 4096 + (((lane & 7) ^ ((row >> 1) & 7)) << 4); }
      dst = lds0 + K2_C + g * 8192; sstep = 128; ahead = 3; nslot = 4;
    } else {
      src = ws + WS_AB + ((size_t)(((ksp >> 1) * 128 + (ksp & 1) * 64 + 2 * (g - 2)) * 16 + eb * 4) << 10);
      voff[0] = lane * 16; voff[1] = lane * 16;
      dst = lds0 + K2_B + (g - 2) * 8192; sstep = 4 * 16 * 1024; ahead = 2; nslot = 3;
    }
    auto issue = [&](int n, int slot) {
      const char* p = src + (size_t)n * sstep;
      const unsigned d = dst + slot * 16384;
      if (g < 2) {
#pragma unroll
        for (int i = 0; i < 8; ++i) dma16(p + (size_t)(i >> 1) * 16 * 4096, voff[i & 1], d + i * 1024);
      } else {
#pragma unroll
        for (int i = 0; i < 8; ++i) dma16(p + (i >> 2) * 16384 + (i & 3) * 1024, voff[0], d + i * 1024);
      }
    };
    for (int i = 0; i < ahead; ++i) issue(i, i);
    const int tm = g & 1, en = g >> 1;
    const int tr0 = 64 * tm + (lane & 31), tr1 = tr0 + 32;
    const unsigned cf0 = K2_C + tr0 * 128 + ((hi ^ ((tr0 >> 1) & 7)) << 4), cf1 = K2_C + tr1 * 128 + ((hi ^ ((tr1 >> 1) & 7)) << 4);
    const unsigned bf = K2_B + en * 2048 + lane * 16;
    f32x16 a00 = {}, a01 = {}, a10 = {}, a11 = {};
    if (g < 2) WAIT_BAR(16); else WAIT_BAR(8);
    asm volatile("s_barrier" ::: "memory");
    int cs = 0, bs = 0;
    for (int n = 0; n < 16; ++n) {
      if (n + ahead < 16) issue(n + ahead, g < 2 ? ((cs + 3) & 3) : (bs == 0 ? 2 : bs - 1));
      const lds_cptr cb_ = (lds_cptr)lds + cs * 16384, bb_ = (lds_cptr)lds + bf + bs * 16384;
#pragma unroll
      for (int ks = 0; ks < 4; ++ks) {
        const bf16x8 c0 = ldsfrag(cb_ + (cf0 ^ (ks << 5))), c1 = ldsfrag(cb_ + (cf1 ^ (ks << 5)));
        const bf16x8 b0 = ldsfrag(bb_ + ks * 4096), b1 = ldsfrag(bb_ + ks * 4096 + 1024);
        a00 = MFMA(b0, c0, a00); a01 = MFMA(b0, c1, a01);
        a10 = MFMA(b1, c0, a10); a11 = MFMA(b1, c1, a11);
      }
      if (g < 2) { if (n < 13) WAIT_BAR(16); else if (n == 13) WAIT_BAR(8); else WAIT_BAR(0); }
      else { if (n < 14) WAIT_BAR(8); else WAIT_BAR(0); }
      cs = (cs + 1) & 3; bs = bs == 2 ? 0 : bs + 1;
    }
    const int tt = tb * 4 + 2 * tm, et = eb * 4 + 2 * en;
    char* pp = ws + WS_P + ((size_t)((ksp * 64 + tt) * 16 + et) << 11) + lane * 16;
    *(u32x4*)(pp) = pack8(a00, 0); *(u32x4*)(pp + 1024) = pack8(a00, 8);
    *(u32x4*)(pp + 2048) = pack8(a10, 0); *(u32x4*)(pp + 2048 + 1024) = pack8(a10, 8);
    *(u32x4*)(pp + 32768) = pack8(a01, 0); *(u32x4*)(pp + 32768 + 1024) = pack8(a01, 8);
    *(u32x4*)(pp + 32768 + 2048) = pack8(a11, 0); *(u32x4*)(pp + 32768 + 2048 + 1024) = pack8(a11, 8);
    STAMP(1, 1);
  }
  STAMP(1, 2);
}

constexpr int K3_LDS = 65536;
__global__ void __launch_bounds__(512, 2) k3_out(const char* __restrict__ wo, const char* __restrict__ ws_, char* __restrict__ out) {
  extern __shared__ __attribute__((aligned(16))) char lds[];
  char* ws = (char*)ws_;
  const int lane = threadIdx.x & 63, w = __builtin_amdgcn_readfirstlane(threadIdx.x >> 6), hi = lane >> 5;
  const int x = blockIdx.x & 7, k = blockIdx.x >> 3;
  const unsigned lds0 = (unsigned)(uintptr_t)lds;
  STAMP(2, 0);
  const int tb = 4 * x + (k >> 3), nb = k & 7;
  {
    const int r = lane >> 3;
    const unsigned vo = r * 1024 + (((lane & 7) ^ (((r >> 1) & 1) << 2)) << 4);
#pragma unroll
    for (int i = 0; i < 8; ++i) dma16(wo + nb * 128 + (size_t)(w * 8 + i) * 8 * 1024, vo, lds0 + (w * 8 + i) * 1024);
  }
  const int kh = w >> 2, tm = w & 1, nn = (w >> 1) & 1;
  const int tt = tb * 2 + tm;
  bf16x8 of[16];
#pragma unroll
  for (int i = 0; i < 16; ++i) of[i] = *(const bf16x8*)(ws + WS_O + ((size_t)(tt * 32 + kh * 16 + i) << 10) + lane * 16);
  f32x16 ys;
  {
    const int et = nb * 2 + nn;
    const char* pp = ws + WS_P + ((size_t)((2 * kh * 64 + tt) * 16 + et) << 11) + lane * 16;
    const u32x4 a0 = *(const u32x4*)pp, a1 = *(const u32x4*)(pp + 1024);
    const u32x4 b0 = *(const u32x4*)(pp + (64 * 16 << 11)), b1 = *(const u32x4*)(pp + (64 * 16 << 11) + 1024);
#pragma unroll
    for (int i = 0; i < 4; ++i) {
      ys[2 * i] = __uint_as_float(a0[i] << 16) + __uint_as_float(b0[i] << 16);
      ys[2 * i + 1] = __uint_as_float(a0[i] & 0xffff0000u) + __uint_as_float(b0[i] & 0xffff0000u);
      ys[8 + 2 * i] = __uint_as_float(a1[i] << 16) + __uint_as_float(b1[i] << 16);
      ys[8 + 2 * i + 1] = __uint_as_float(a1[i] & 0xffff0000u) + __uint_as_float(b1[i] & 0xffff0000u);
    }
  }
  asm volatile("s_waitcnt vmcnt(0)\n\ts_barrier" ::: "memory");
  const int q4 = (lane & 15) >> 2, p4 = lane & 3, blk = (lane >> 4) & 1;
  const lds_cptr wf = (lds_cptr)lds + (kh * 256 + 4 * hi + q4) * 128 + (((4 * nn + 2 * blk + (p4 >> 1)) ^ ((q4 >> 1) << 2)) << 4) + (p4 & 1) * 8;
  f32x16 acc = {};
#pragma unroll
  for (int i = 0; i < 16; ++i) {
    const bf16x8 a = cat8(ldstr(wf + i * 2048), ldstr(wf + i * 2048 + 1024));
    acc = MFMA(a, of[i], acc);
  }
  __syncthreads();
  if (kh == 1) {
    LAS float* r = (LAS float*)((LAS char*)lds + (w & 3) * 8192);
#pragma unroll
    for (int i = 0; i < 4; ++i) {
      *(LAS f32x4*)(r + i * 256 + lane * 4) = (f32x4){acc[4 * i], acc[4 * i + 1], acc[4 * i + 2], acc[4 * i + 3]};
      *(LAS f32x4*)(r + 1024 + i * 256 + lane * 4) = (f32x4){ys[4 * i], ys[4 * i + 1], ys[4 * i + 2], ys[4 * i + 3]};
    }
  }
  __syncthreads();
  if (kh == 0) {
    const LAS float* r = (const LAS float*)((LAS char*)lds + (w & 3) * 8192);
#pragma unroll
    for (int i = 0; i < 4; ++i) {
      const f32x4 u = *(const LAS f32x4*)(r + i * 256 + lane * 4);
      const f32x4 v = *(const LAS f32x4*)(r + 1024 + i * 256 + lane * 4);
#pragma unroll
      for (int j = 0; j < 4; ++j) { acc[4 * i + j] += u[j]; ys[4 * i + j] += v[j]; }
    }
    const int row = tt * 32 + (lane & 31);
    char* op = out + (size_t)row * 2048 + (nb * 64 + nn * 32 + hi * 8) * 2;
    *(u32x4*)(op) = natorder(pack8(acc, 0));
    *(u32x4*)(op + 32) = natorder(pack8(acc, 8));
    *(u32x4*)(op + 1024) = natorder(pack8(ys, 0));
    *(u32x4*)(op + 1024 + 32) = natorder(pack8(ys, 8));
  }
  STAMP(2, 2);
}

extern "C" void kernel_launch(void* const* d_in, const int* in_sizes, int n_in, void* d_out, int out_size, void* d_ws, size_t ws_size, hipStream_t stream) {
  const char* xa = (const char*)d_in[0];
  const char* wqkv = (const char*)d_in[1];
  const char* wo = (const char*)d_in[2];
  const char* xb = (const char*)d_in[3];
  const char* cre = (const char*)d_in[4];
  const char* cim = (const char*)d_in[5];
  const char* dre = (const char*)d_in[6];
  const char* dim = (const char*)d_in[7];
  char* ws = (char*)d_ws;
  hipFuncSetAttribute((const void*)k1_proj, hipFuncAttributeMaxDynamicSharedMemorySize, K1_LDS);
  hipFuncSetAttribute((const void*)k2_attn, hipFuncAttributeMaxDynamicSharedMemorySize, K2_LDS);
  hipFuncSetAttribute((const void*)k3_out, hipFuncAttributeMaxDynamicSharedMemorySize, K3_LDS);
  k1_proj<<<256, 512, K1_LDS, stream>>>(xa, wqkv, xb, dre, dim, ws);
  k2_attn<<<256, K2_THREADS, K2_LDS, stream>>>(cre, cim, ws);
  k3_out<<<256, 512, K3_LDS, stream>>>(wo, ws, (char*)d_out);
}
```

```cpp
#include <hip/hip_runtime.h>
#include <hip/hip_bf16.h>
#include <stdint.h>

typedef __attribute__((ext_vector_type(8))) short bf16x8;
typedef __attribute__((ext_vector_type(4))) short s16x4;
typedef __attribute__((ext_vector_type(16))) float f32x16;
typedef __attribute__((ext_vector_type(4))) unsigned u32x4;
typedef __attribute__((ext_vector_type(2))) unsigned u32x2;
typedef __attribute__((ext_vector_type(4))) float f32x4;
#define LAS __attribute__((address_space(3)))
typedef LAS const char* lds_cptr;

#define SBAR() __builtin_amdgcn_sched_barrier(0)
#define PIN(x) asm volatile("" : "+v"(x))
#define MFMA(a, b, c) __builtin_amdgcn_mfma_f32_32x32x16_bf16(a, b, c, 0, 0, 0)

constexpr int S = 2048, DM = 512, NH = 32;
constexpr size_t WS_Q = 0;
constexpr size_t WS_K = 2u << 20;
constexpr size_t WS_V = 4u << 20;
constexpr size_t WS_AB = 6u << 20;
constexpr size_t WS_O = 10u << 20;
constexpr size_t WS_P = 12u << 20;
#define STEPSTAMP(n)
#define STAMP(kid, i)
#define STAMPC(kid, i)

typedef __attribute__((ext_vector_type(2))) float f32x2;
typedef __attribute__((ext_vector_type(2))) __bf16 bf16x2;
__device__ __forceinline__ unsigned cvtpk(float lo, float hi) { const f32x2 v = {lo, hi}; return __builtin_bit_cast(unsigned, __builtin_convertvector(v, bf16x2)); }
__device__ __forceinline__ void dma16(const void* sbase, unsigned voff, unsigned ldsaddr) {
  asm volatile("s_mov_b32 m0, %2\n\ts_nop 0\n\tglobal_load_lds_dwordx4 %0, %1" ::"v"(voff), "s"(sbase), "s"(ldsaddr) : "memory");
}
#define WAIT_BAR(N) asm volatile("s_waitcnt vmcnt(" #N ") lgkmcnt(0)\n\ts_barrier" ::: "memory")
__device__ __forceinline__ bf16x8 ldsfrag(lds_cptr p) { return *(LAS const bf16x8*)p; }
typedef short v4i16_t __attribute__((ext_vector_type(4)));
__device__ __forceinline__ s16x4 ldstr(lds_cptr p) { return __builtin_bit_cast(s16x4, __builtin_amdgcn_ds_read_tr16_b64_v4i16((LAS v4i16_t*)p)); }
__device__ __forceinline__ bf16x8 cat8(s16x4 a, s16x4 b) { return (bf16x8){a[0], a[1], a[2], a[3], b[0], b[1], b[2], b[3]}; }
__device__ __forceinline__ u32x4 pack8(const f32x16& c, int r0, float sc) {
  return (u32x4){cvtpk(c[r0] * sc, c[r0 + 1] * sc), cvtpk(c[r0 + 2] * sc, c[r0 + 3] * sc), cvtpk(c[r0 + 4] * sc, c[r0 + 5] * sc), cvtpk(c[r0 + 6] * sc, c[r0 + 7] * sc)};
}
__device__ __forceinline__ u32x4 pack8(const f32x16& c, int r0) {
  return (u32x4){cvtpk(c[r0], c[r0 + 1]), cvtpk(c[r0 + 2], c[r0 + 3]), cvtpk(c[r0 + 4], c[r0 + 5]), cvtpk(c[r0 + 6], c[r0 + 7])};
}
__device__ __forceinline__ u32x4 natorder(u32x4 w) {
  auto a = __builtin_amdgcn_permlane32_swap(w[0], w[2], false, false);
  auto b = __builtin_amdgcn_permlane32_swap(w[1], w[3], false, false);
  return (u32x4){a[0], b[0], a[1], b[1]};
}

#define WS_T0 (40u << 20)
#define PROBE_SET()
#define PROBE_WAIT(T)
constexpr int K1_SLOT = 40960, K1_LDS = 4 * K1_SLOT;
#define RA 2
#define ST_AUX 17
__device__ __forceinline__ void st16(__amdgpu_buffer_rsrc_t r, unsigned off, u32x4 v) { __builtin_amdgcn_raw_buffer_store_b128(v, r, off, 0, ST_AUX); }
__device__ __forceinline__ __amdgpu_buffer_rsrc_t mkrsrc(const void* p, unsigned bytes) { return __builtin_amdgcn_make_buffer_rsrc((void*)p, (short)0, (int)bytes, 0x00020000); }
#define WAITV(N) asm volatile("s_waitcnt vmcnt(" #N ")" ::: "memory")
#define WAITL() asm volatile("s_waitcnt lgkmcnt(0)" ::: "memory")
#define BAR() asm volatile("s_barrier" ::: "memory")

#define dma16k1 dma16
__global__ void __launch_bounds__(512, 2) k1_proj(const char* __restrict__ xa, const char* __restrict__ wqkv, const char* __restrict__ xb,
                                                  const char* __restrict__ dre, const char* __restrict__ dim, char* __restrict__ ws) {
  extern __shared__ __attribute__((aligned(16))) char lds[];
  const int lane = threadIdx.x & 63, w = __builtin_amdgcn_readfirstlane(threadIdx.x >> 6), hi = lane >> 5;
  const int x = blockIdx.x & 7, k = blockIdx.x >> 3;
  const int rb = (x & 3) * 4 + (k & 3), cb = (x >> 2) * 4 + ((k >> 2) & 3);
  const unsigned lds0 = (unsigned)(uintptr_t)lds;
  const bool qkv = k < 16;
  PROBE_SET();
  STAMP(0, 0);
  if (w >= 4) {
    const int p = w - 4;
    const int rot = (k + x) & 7;
    unsigned vx[2];
#pragma unroll
    for (int j = 0; j < 2; ++j) { const int row = 8 * j + (lane >> 3); vx[j] = row * 1024 + (((lane & 7) ^ ((row >> 1) & 7)) << 4); }
    if (qkv) {
      const char* xs = xa + (size_t)(rb * 128 + p * 32) * 1024;
      const char* wsrc = wqkv + cb * 128 + (size_t)p * 16 * 3072;
      const int wr = lane >> 3;
      const unsigned vw = wr * 3072 + (((lane & 7) ^ (((wr >> 1) & 1) << 2)) << 4);
      auto issue = [&](int s) {
        const int ks = (s + rot) & 7;
        const unsigned sl = lds0 + (s & 3) * K1_SLOT;
        const char* xp = xs + ks * 128;
        const char* wp = wsrc + (size_t)ks * 64 * 3072;
#pragma unroll
        for (int i = 0; i < 4; ++i) dma16k1(xp + (i >> 1) * 16384, vx[i & 1], sl + p * 4096 + i * 1024);
#pragma unroll
        for (int i = 0; i < 6; ++i) dma16k1(wp + (i >> 1) * 1024 + (i & 1) * 8 * 3072, vw, sl + 16384 + (i >> 1) * 8192 + p * 2048 + (i & 1) * 1024);
      };
      issue(0); issue(1);
      WAITV(10); BAR();
      issue(2);
      for (int s = 1; s < 8; ++s) {
        if (s < 7) WAITV(10); else WAITV(0);
        BAR();
        if (s + RA < 8) issue(s + RA);
      }
    } else {
      const char* xs = xb + (size_t)(rb * 128 + p * 32) * 1024;
      const char* ds = ((cb >> 2) ? dim : dre) + (size_t)((cb & 3) * 128 + p * 32) * 1024;
      auto issue = [&](int s) {
        const int ks = (s + rot) & 7;
        const unsigned sl = lds0 + (s & 3) * K1_SLOT;
#pragma unroll
        for (int i = 0; i < 4; ++i) dma16k1(xs + ks * 128 + (i >> 1) * 16384, vx[i & 1], sl + p * 4096 + i * 1024);
#pragma unroll
        for (int i = 0; i < 4; ++i) dma16k1(ds + ks * 128 + (i >> 1) * 16384, vx[i & 1], sl + 16384 + p * 4096 + i * 1024);
      };
      issue(0); issue(1);
      WAITV(8); BAR();
      issue(2);
      for (int s = 1; s < 8; ++s) {
        if (s < 7) WAITV(8); else WAITV(0);
        BAR();
        if (s + RA < 8) issue(s + RA);
      }
    }
    return;
  }
  const __amdgpu_buffer_rsrc_t wsr = mkrsrc(ws, WS_P);
  const int tm = w & 1, ch = w >> 1;
  const int frow = 64 * tm + (lane & 31);
  const unsigned xa0 = lds0 + frow * 128 + ((hi ^ ((frow >> 1) & 7)) << 4);
  auto ldx = [&](unsigned so, int ks, int t) { return ldsfrag((lds_cptr)(uintptr_t)((xa0 ^ (ks << 5)) + so) + t * 4096); };
  if (qkv) {
    const int q4 = (lane & 15) >> 2, p4 = lane & 3, blk = (lane >> 4) & 1;
    const unsigned wa0 = lds0 + 16384 + (8 * hi + q4) * 128 + (((4 * ch + 2 * blk + (p4 >> 1)) ^ ((q4 >> 1) << 2)) << 4) + (p4 & 1) * 8;
    auto ldw = [&](unsigned so, int ks, int m) { const lds_cptr wp = (lds_cptr)(uintptr_t)(wa0 + so) + ks * 2048 + m * 8192; return cat8(ldstr(wp), ldstr(wp + 512)); };
    f32x16 aq0 = {}, aq1 = {}, ak0 = {}, ak1 = {}, av0 = {}, av1 = {};
    bf16x8 x0, x1, wq, wk, wv, nx0, nx1, nwq, nwk, nwv;
    BAR();
    STAMP(0, 8); STAMPC(0, 24);
    x0 = ldx(0, 0, 0); wq = ldw(0, 0, 0); x1 = ldx(0, 0, 1); wk = ldw(0, 0, 1); wv = ldw(0, 0, 2);
#define QSUB(so, ks, SYNC, LOAD) do { \
      if (SYNC) WAITL(); SBAR(); \
      aq0 = MFMA(wq, x0, aq0); SBAR(); \
      if (SYNC) BAR(); SBAR(); \
      if (LOAD) { nx0 = ldx(so, ks, 0); nwq = ldw(so, ks, 0); } SBAR(); \
      aq1 = MFMA(wq, x1, aq1); SBAR(); \
      if (LOAD) { nx1 = ldx(so, ks, 1); nwk = ldw(so, ks, 1); } SBAR(); \
      ak0 = MFMA(wk, x0, ak0); SBAR(); \
      if (LOAD) { nwv = ldw(so, ks, 2); } SBAR(); \
      ak1 = MFMA(wk, x1, ak1); SBAR(); \
      av0 = MFMA(x0, wv, av0); SBAR(); \
      av1 = MFMA(x1, wv, av1); SBAR(); \
      if (LOAD) { x0 = nx0; x1 = nx1; wq = nwq; wk = nwk; wv = nwv; } \
    } while (0)
#pragma unroll
    for (int s = 0; s < 8; ++s) {
      const unsigned so = (s & 3) * K1_SLOT, sn = ((s + 1) & 3) * K1_SLOT;
      QSUB(so, 1, false, true); QSUB(so, 2, false, true); QSUB(so, 3, false, true);
      QSUB(sn, 0, s < 7, s < 7);
    }
    STAMP(0, 1); STAMPC(0, 25);
    constexpr float QS = 0.25f * 1.4426950408889634f;
    const int h0 = 4 * cb + 2 * ch, tile = rb * 4 + 2 * tm;
    const unsigned qo = WS_Q + ((h0 * 64 + tile) << 10) + lane * 16;
    st16(wsr, qo, pack8(aq0, 0, QS)); st16(wsr, qo + 65536, pack8(aq0, 8, QS));
    st16(wsr, qo + 1024, pack8(aq1, 0, QS)); st16(wsr, qo + 1024 + 65536, pack8(aq1, 8, QS));
    const unsigned ko = qo + (unsigned)(WS_K - WS_Q);
    st16(wsr, ko, pack8(ak0, 0)); st16(wsr, ko + 65536, pack8(ak0, 8));
    st16(wsr, ko + 1024, pack8(ak1, 0)); st16(wsr, ko + 1024 + 65536, pack8(ak1, 8));
    const unsigned vo = WS_V + ((h0 + ((lane >> 4) & 1)) << 16) + tile * 1024 + (hi * 16 + (lane & 15)) * 16;
    st16(wsr, vo, pack8(av0, 0)); st16(wsr, vo + 512, pack8(av0, 8));
    st16(wsr, vo + 1024, pack8(av1, 0)); st16(wsr, vo + 1024 + 512, pack8(av1, 8));
  } else {
    const int er = 64 * ch + (lane & 31);
    const unsigned da0 = lds0 + 16384 + er * 128 + ((hi ^ ((er >> 1) & 7)) << 4);
    auto ldd = [&](unsigned so, int ks, int t) { return ldsfrag((lds_cptr)(uintptr_t)((da0 ^ (ks << 5)) + so) + t * 4096); };
    f32x16 a00 = {}, a01 = {}, a10 = {}, a11 = {};
    bf16x8 x0, x1, d0, d1, nx0, nx1, nd0, nd1;
    BAR();
    STAMP(0, 17);
    x0 = ldx(0, 0, 0); d0 = ldd(0, 0, 0); d1 = ldd(0, 0, 1); x1 = ldx(0, 0, 1);
#define DSUB(so, ks, SYNC, LOAD) do { \
      if (SYNC) WAITL(); SBAR(); \
      a00 = MFMA(x0, d0, a00); SBAR(); \
      if (SYNC) BAR(); SBAR(); \
      if (LOAD) { nx0 = ldx(so, ks, 0); nd0 = ldd(so, ks, 0); } SBAR(); \
      a01 = MFMA(x0, d1, a01); SBAR(); \
      if (LOAD) { nd1 = ldd(so, ks, 1); nx1 = ldx(so, ks, 1); } SBAR(); \
      a10 = MFMA(x1, d0, a10); SBAR(); \
      a11 = MFMA(x1, d1, a11); SBAR(); \
      if (LOAD) { x0 = nx0; x1 = nx1; d0 = nd0; d1 = nd1; } \
    } while (0)
#pragma unroll
    for (int s = 0; s < 8; ++s) {
      const unsigned so = (s & 3) * K1_SLOT, sn = ((s + 1) & 3) * K1_SLOT;
      DSUB(so, 1, false, true); DSUB(so, 2, false, true); DSUB(so, 3, false, true);
      DSUB(sn, 0, s < 7, s < 7);
    }
    STAMP(0, 3);
    const int part = cb >> 2;
    const float sg = part ? -1.f : 1.f;
    const int et = (cb & 3) * 4 + 2 * ch, s16 = rb * 8 + tm * 4;
    const unsigned op = WS_AB + (((part * 128 + s16) * 16 + et) << 10) + lane * 16;
    st16(wsr, op, natorder(pack8(a00, 0, sg))); st16(wsr, op + 16384, natorder(pack8(a00, 8, sg)));
    st16(wsr, op + 1024, natorder(pack8(a01, 0, sg))); st16(wsr, op + 1024 + 16384, natorder(pack8(a01, 8, sg)));
    st16(wsr, op + 32768, natorder(pack8(a10, 0, sg))); st16(wsr, op + 32768 + 16384, natorder(pack8(a10, 8, sg)));
    st16(wsr, op + 32768 + 1024, natorder(pack8(a11, 0, sg))); st16(wsr, op + 32768 + 1024 + 16384, natorder(pack8(a11, 8, sg)));
  }
  STAMP(0, 2);
}

constexpr int K2_KR = 0, K2_VR = 16384, K2_CONST = 32768, K2_C = 36864, K2_B = K2_C + 4 * 16384, K2_LDS = K2_B + 3 * 16384;
#define K2_GEMM 1
constexpr int K2_THREADS = K2_GEMM ? 768 : 512;

__device__ __forceinline__ void attn_exact(const char* ws, int h, int qt, char* optr, int lane) {
  const int hi = lane >> 5;
  const bf16x8 qf = *(const bf16x8*)(ws + WS_Q + ((size_t)(h * 64 + qt) << 10) + lane * 16);
  f32x16 o = {};
  float m = -INFINITY;
  const bool isv = (lane & 31) < 16;
  const short one = ((lane & 31) == 16 || (lane & 31) == 20) ? (short)0x3f80 : (short)0;
  const bf16x8 cst = {one, one, one, one, one, one, one, one};
  for (int t = 0; t < 64; ++t) {
    const bf16x8 kf = *(const bf16x8*)(ws + WS_K + ((size_t)(h * 64 + t) << 10) + lane * 16);
    const char* vp = ws + WS_V + ((size_t)h << 16) + t * 1024 + (hi * 16 + (lane & 15)) * 16;
    bf16x8 v0 = *(const bf16x8*)vp, v1 = *(const bf16x8*)(vp + 512);
    v0 = isv ? v0 : cst; v1 = isv ? v1 : cst;
    f32x16 s = {};
    s = MFMA(kf, qf, s);
    float mx = s[0];
#pragma unroll
    for (int r = 1; r < 16; ++r) mx = fmaxf(mx, s[r]);
    mx = fmaxf(mx, __shfl_xor(mx, 32));
    const float mn = fmaxf(m, mx), al = __builtin_amdgcn_exp2f(m - mn);
    m = mn;
#pragma unroll
    for (int r = 0; r < 16; ++r) { o[r] *= al; s[r] = __builtin_amdgcn_exp2f(s[r] - mn); }
    o = MFMA(v0, __builtin_bit_cast(bf16x8, pack8(s, 0)), o);
    o = MFMA(v1, __builtin_bit_cast(bf16x8, pack8(s, 8)), o);
  }
  *(u32x4*)optr = pack8(o, 0, 1.f / o[8]);
}

__global__ void __launch_bounds__(K2_THREADS, K2_GEMM ? 3 : 2) k2_attn(const char* __restrict__ cre, const char* __restrict__ cim, char* __restrict__ ws) {
  extern __shared__ __attribute__((aligned(16))) char lds[];
  const int lane = threadIdx.x & 63, w = __builtin_amdgcn_readfirstlane(threadIdx.x >> 6), hi = lane >> 5;
  const int x = blockIdx.x & 7, k = blockIdx.x >> 3;
  const unsigned lds0 = (unsigned)(uintptr_t)lds;
  STAMP(1, 0);
  if (w < 8) {
    const int h = 4 * x + (k >> 3), qt = (k & 7) * 8 + w;
    const char* src = ws + (w < 4 ? WS_K : WS_V) + ((size_t)h << 16) + (w & 3) * 1024;
    const unsigned dst = lds0 + (w < 4 ? K2_KR : K2_VR) + (w & 3) * 1024;
    const int ahead = w < 4 ? 3 : 2;
    const unsigned vl = lane * 16;
    for (int i = 0; i < ahead; ++i) dma16(src + i * 4096, vl, dst + i * 4096);
    if (threadIdx.x < 256) {
      const int c = threadIdx.x;
      const unsigned one = ((c & 31) == 0 || (c & 31) == 4) ? 0x3f803f80u : 0u;
      *(LAS u32x4*)((LAS char*)lds + K2_CONST + c * 16) = (u32x4){one, one, one, one};
    }
    const bf16x8 qf = *(const bf16x8*)(ws + WS_Q + ((size_t)(h * 64 + qt) << 10) + lane * 16);
    const int r32 = lane & 31;
    const int cchunk = r32 < 20 ? r32 - 16 : (r32 < 28 ? (r32 == 20 ? 4 : 5) : 1);
    const unsigned vbase = r32 < 16 ? (unsigned)(K2_VR + (hi * 16 + r32) * 16) : (unsigned)(K2_CONST + cchunk * 16);
    const unsigned vstep = r32 < 16 ? 4096u : 0u;
    const lds_cptr kp = (lds_cptr)lds + K2_KR + lane * 16;
    if (w < 4) WAIT_BAR(2); else WAIT_BAR(1);
    f32x16 cm, sA, sB, o = {};
    {
      f32x16 z = {};
      sA = MFMA(ldsfrag(kp), qf, z);
      float mx = sA[0];
#pragma unroll
      for (int r = 1; r < 16; ++r) mx = fmaxf(mx, sA[r]);
      auto sw = __builtin_amdgcn_permlane32_swap(__float_as_uint(mx), __float_as_uint(mx), false, false);
      mx = fmaxf(__uint_as_float(sw[0]), __uint_as_float(sw[1]));
      const float nm = -(mx + 48.f);
#pragma unroll
      for (int r = 0; r < 16; ++r) { cm[r] = nm; sA[r] += nm; }
    }
    PIN(cm);
    u32x4 p0 = {}, p1 = {};
    bf16x8 kf = ldsfrag(kp + 1024), vf0, vf1;
    lds_cptr vp = (lds_cptr)lds + vbase;
    vf0 = ldsfrag(vp);
    vf1 = ldsfrag(vp + 512);
    if (w < 4) WAIT_BAR(1); else WAIT_BAR(0);
#define EX(v) __builtin_amdgcn_exp2f(v)
#define PVM(A, B, C) MFMA(A, B, C)
#define SM(A, B, C, OLD) MFMA(A, B, C)
#define LDF(P, OLD) ldsfrag(P)
#define TILE(CUR, NXT, KNEXT, V0NEXT, V1CUR, LASTT) do { \
      CUR[0] = EX(CUR[0]); CUR[1] = EX(CUR[1]); CUR[2] = EX(CUR[2]); CUR[3] = EX(CUR[3]); \
      p0[0] = cvtpk(CUR[0], CUR[1]); p0[1] = cvtpk(CUR[2], CUR[3]); PIN(p0); SBAR(); \
      o = PVM(vf1, __builtin_bit_cast(bf16x8, p1), o); SBAR(); \
      vf1 = LDF(V1CUR, vf1); SBAR(); \
      CUR[4] = EX(CUR[4]); CUR[5] = EX(CUR[5]); CUR[6] = EX(CUR[6]); CUR[7] = EX(CUR[7]); \
      p0[2] = cvtpk(CUR[4], CUR[5]); p0[3] = cvtpk(CUR[6], CUR[7]); PIN(p0); SBAR(); \
      NXT = SM(kf, qf, cm, NXT); SBAR(); \
      kf = LDF(KNEXT, kf); SBAR(); \
      CUR[8] = EX(CUR[8]); CUR[9] = EX(CUR[9]); CUR[10] = EX(CUR[10]); CUR[11] = EX(CUR[11]); \
      p1[0] = cvtpk(CUR[8], CUR[9]); p1[1] = cvtpk(CUR[10], CUR[11]); PIN(p1); SBAR(); \
      o = PVM(vf0, __builtin_bit_cast(bf16x8, p0), o); SBAR(); \
      if (!(LASTT)) { vf0 = LDF(V0NEXT, vf0); SBAR(); } \
      CUR[12] = EX(CUR[12]); CUR[13] = EX(CUR[13]); CUR[14] = EX(CUR[14]); CUR[15] = EX(CUR[15]); \
      p1[2] = cvtpk(CUR[12], CUR[13]); p1[3] = cvtpk(CUR[14], CUR[15]); PIN(p1); SBAR(); \
    } while (0)
#define STEP(n, sl, sl1, LAST) do { \
      STEPSTAMP(n); \
      if ((n) + ahead < 16) dma16(src + ((n) + ahead) * 4096, vl, dst + (((n) + ahead) & 3) * 4096); \
      const lds_cptr k0 = kp + (sl) * 4096, k1 = kp + (sl1) * 4096; \
      const lds_cptr v0 = (lds_cptr)lds + vbase + (sl) * vstep; \
      const lds_cptr v1 = (lds_cptr)lds + vbase + (sl1) * vstep; \
      TILE(sA, sB, k0 + 2048, v0 + 1024, v0 + 512, 0); \
      TILE(sB, sA, k0 + 3072, v0 + 2048, v0 + 1536, 0); \
      TILE(sA, sB, k1, v0 + 3072, v0 + 2560, 0); \
        \
      TILE(sB, sA, k1 + 1024, v0 + 3072, v0 + 3584, 1); \
      if ((n) < 13) WAIT_BAR(1); else WAIT_BAR(0); \
      vf0 = LDF(v1, vf0); \
    } while (0)
    STAMP(1, 3); STAMPC(1, 24);
    for (int n = 0; n < 16; n += 4) {
      STEP(n, 0, 1, 0); STEP(n + 1, 1, 2, 0); STEP(n + 2, 2, 3, 0); STEP(n + 3, 3, 0, 0);
    }
    o = MFMA(vf1, __builtin_bit_cast(bf16x8, p1), o);
    STAMP(1, 1); STAMPC(1, 25); STEPSTAMP(16);
    char* optr = ws + WS_O + ((size_t)(qt * 32 + h) << 10) + lane * 16;
    const float l = o[8];
    const bool bad = !(l > 0x1p-100f && l < 0x1p100f);
    if (__builtin_expect(__any(bad), 0)) attn_exact(ws, h, qt, optr, lane);
    else *(u32x4*)optr = pack8(o, 0, 1.f / l);
  }
  else {
    const int g = w - 8;
    const int ksp = x >> 1, tb = (x & 1) * 8 + (k >> 2), eb = k & 3;
#define GPRIO 2
#define GSLEEP 0
    __builtin_amdgcn_s_setprio(GPRIO);
    const char* src;
    unsigned voff[2];
    unsigned dst;
    size_t sstep;
    int ahead, nslot;
    if (g < 2) {
      src = ((ksp >> 1) ? cim : cre) + (size_t)(tb * 128 + g * 64) * 4096 + (ksp & 1) * 2048;
#pragma unroll
      for (int j = 0; j < 2; ++j) { const int row = 8 * j + (lane >> 3); voff[j] = row * 4096 + (((lane & 7) ^ ((row >> 1) & 7)) << 4); }
      dst = lds0 + K2_C + g * 8192; sstep = 128; ahead = 3; nslot = 4;
    } else {
      src = ws + WS_AB + ((size_t)(((ksp >> 1) * 128 + (ksp & 1) * 64 + 2 * (g - 2)) * 16 + eb * 4) << 10);
      voff[0] = lane * 16; voff[1] = lane * 16;
      dst = lds0 + K2_B + (g - 2) * 8192; sstep = 4 * 16 * 1024; ahead = 2; nslot = 3;
    }
    auto issue = [&](int n, int slot) {
      const char* p = src + (size_t)n * sstep;
      const unsigned d = dst + slot * 16384;
      if (g < 2) {
#pragma unroll
        for (int i = 0; i < 8; ++i) dma16(p + (size_t)(i >> 1) * 16 * 4096, voff[i & 1], d + i * 1024);
      } else {
#pragma unroll
        for (int i = 0; i < 8; ++i) dma16(p + (i >> 2) * 16384 + (i & 3) * 1024, voff[0], d + i * 1024);
      }
    };
    for (int i = 0; i < ahead; ++i) issue(i, i);
    const int tm = g & 1, en = g >> 1;
    const int tr0 = 64 * tm + (lane & 31), tr1 = tr0 + 32;
    const unsigned cf0 = K2_C + tr0 * 128 + ((hi ^ ((tr0 >> 1) & 7)) << 4), cf1 = K2_C + tr1 * 128 + ((hi ^ ((tr1 >> 1) & 7)) << 4);
    const unsigned bf = K2_B + en * 2048 + lane * 16;
    f32x16 a00 = {}, a01 = {}, a10 = {}, a11 = {};
    if (g < 2) WAIT_BAR(16); else WAIT_BAR(8);
    asm volatile("s_barrier" ::: "memory");
    int cs = 0, bs = 0;
    for (int n = 0; n < 16; ++n) {
      if (n + ahead < 16) issue(n + ahead, g < 2 ? ((cs + 3) & 3) : (bs == 0 ? 2 : bs - 1));
      const lds_cptr cb_ = (lds_cptr)lds + cs * 16384, bb_ = (lds_cptr)lds + bf + bs * 16384;
#pragma unroll
      for (int ks = 0; ks < 4; ++ks) {
        const bf16x8 c0 = ldsfrag(cb_ + (cf0 ^ (ks << 5))), c1 = ldsfrag(cb_ + (cf1 ^ (ks << 5)));
        const bf16x8 b0 = ldsfrag(bb_ + ks * 4096), b1 = ldsfrag(bb_ + ks * 4096 + 1024);
        a00 = MFMA(b0, c0, a00); a01 = MFMA(b0, c1, a01);
        a10 = MFMA(b1, c0, a10); a11 = MFMA(b1, c1, a11);
      }
      if (g < 2) { if (n < 13) WAIT_BAR(16); else if (n == 13) WAIT_BAR(8); else WAIT_BAR(0); }
      else { if (n < 14) WAIT_BAR(8); else WAIT_BAR(0); }
      cs = (cs + 1) & 3; bs = bs == 2 ? 0 : bs + 1;
    }
    const int tt = tb * 4 + 2 * tm, et = eb * 4 + 2 * en;
    char* pp = ws + WS_P + ((size_t)((ksp * 64 + tt) * 16 + et) << 11) + lane * 16;
    *(u32x4*)(pp) = pack8(a00, 0); *(u32x4*)(pp + 1024) = pack8(a00, 8);
    *(u32x4*)(pp + 2048) = pack8(a10, 0); *(u32x4*)(pp + 2048 + 1024) = pack8(a10, 8);
    *(u32x4*)(pp + 32768) = pack8(a01, 0); *(u32x4*)(pp + 32768 + 1024) = pack8(a01, 8);
    *(u32x4*)(pp + 32768 + 2048) = pack8(a11, 0); *(u32x4*)(pp + 32768 + 2048 + 1024) = pack8(a11, 8);
    STAMP(1, 1);
  }
  STAMP(1, 2);
}

constexpr int K3_LDS = 65536;
__global__ void __launch_bounds__(512, 2) k3_out(const char* __restrict__ wo, const char* __restrict__ ws_, char* __restrict__ out) {
  extern __shared__ __attribute__((aligned(16))) char lds[];
  char* ws = (char*)ws_;
  const int lane = threadIdx.x & 63, w = __builtin_amdgcn_readfirstlane(threadIdx.x >> 6), hi = lane >> 5;
  const int x = blockIdx.x & 7, k = blockIdx.x >> 3;
  const unsigned lds0 = (unsigned)(uintptr_t)lds;
  STAMP(2, 0);
  const int tb = 4 * x + (k >> 3), nb = k & 7;
  {
    const int r = lane >> 3;
    const unsigned vo = r * 1024 + (((lane & 7) ^ (((r >> 1) & 1) << 2)) << 4);
#pragma unroll
    for (int i = 0; i < 8; ++i) dma16(wo + nb * 128 + (size_t)(w * 8 + i) * 8 * 1024, vo, lds0 + (w * 8 + i) * 1024);
  }
  const int kh = w >> 2, tm = w & 1, nn = (w >> 1) & 1;
  const int tt = tb * 2 + tm;
  bf16x8 of[16];
#pragma unroll
  for (int i = 0; i < 16; ++i) of[i] = *(const bf16x8*)(ws + WS_O + ((size_t)(tt * 32 + kh * 16 + i) << 10) + lane * 16);
  f32x16 ys;
  {
    const int et = nb * 2 + nn;
    const char* pp = ws + WS_P + ((size_t)((2 * kh * 64 + tt) * 16 + et) << 11) + lane * 16;
    const u32x4 a0 = *(const u32x4*)pp, a1 = *(const u32x4*)(pp + 1024);
    const u32x4 b0 = *(const u32x4*)(pp + (64 * 16 << 11)), b1 = *(const u32x4*)(pp + (64 * 16 << 11) + 1024);
#pragma unroll
    for (int i = 0; i < 4; ++i) {
      ys[2 * i] = __uint_as_float(a0[i] << 16) + __uint_as_float(b0[i] << 16);
      ys[2 * i + 1] = __uint_as_float(a0[i] & 0xffff0000u) + __uint_as_float(b0[i] & 0xffff0000u);
      ys[8 + 2 * i] = __uint_as_float(a1[i] << 16) + __uint_as_float(b1[i] << 16);
      ys[8 + 2 * i + 1] = __uint_as_float(a1[i] & 0xffff0000u) + __uint_as_float(b1[i] & 0xffff0000u);
    }
  }
  asm volatile("s_waitcnt vmcnt(0)\n\ts_barrier" ::: "memory");
  const int q4 = (lane & 15) >> 2, p4 = lane & 3, blk = (lane >> 4) & 1;
  const lds_cptr wf = (lds_cptr)lds + (kh * 256 + 4 * hi + q4) * 128 + (((4 * nn + 2 * blk + (p4 >> 1)) ^ ((q4 >> 1) << 2)) << 4) + (p4 & 1) * 8;
  f32x16 acc = {};
#pragma unroll
  for (int i = 0; i < 16; ++i) {
    const bf16x8 a = cat8(ldstr(wf + i * 2048), ldstr(wf + i * 2048 + 1024));
    acc = MFMA(a, of[i], acc);
  }
  __syncthreads();
  if (kh == 1) {
    LAS float* r = (LAS float*)((LAS char*)lds + (w & 3) * 8192);
#pragma unroll
    for (int i = 0; i < 4; ++i) {
      *(LAS f32x4*)(r + i * 256 + lane * 4) = (f32x4){acc[4 * i], acc[4 * i + 1], acc[4 * i + 2], acc[4 * i + 3]};
      *(LAS f32x4*)(r + 1024 + i * 256 + lane * 4) = (f32x4){ys[4 * i], ys[4 * i + 1], ys[4 * i + 2], ys[4 * i + 3]};
    }
  }
  __syncthreads();
  if (kh == 0) {
    const LAS float* r = (const LAS float*)((LAS char*)lds + (w & 3) * 8192);
#pragma unroll
    for (int i = 0; i < 4; ++i) {
      const f32x4 u = *(const LAS f32x4*)(r + i * 256 + lane * 4);
      const f32x4 v = *(const LAS f32x4*)(r + 1024 + i * 256 + lane * 4);
#pragma unroll
      for (int j = 0; j < 4; ++j) { acc[4 * i + j] += u[j]; ys[4 * i + j] += v[j]; }
    }
    const int row = tt * 32 + (lane & 31);
    char* op = out + (size_t)row * 2048 + (nb * 64 + nn * 32 + hi * 8) * 2;
    *(u32x4*)(op) = natorder(pack8(acc, 0));
    *(u32x4*)(op + 32) = natorder(pack8(acc, 8));
    *(u32x4*)(op + 1024) = natorder(pack8(ys, 0));
    *(u32x4*)(op + 1024 + 32) = natorder(pack8(ys, 8));
  }
  STAMP(2, 2);
}

extern "C" void kernel_launch(void* const* d_in, const int* in_sizes, int n_in, void* d_out, int out_size, void* d_ws, size_t ws_size, hipStream_t stream) {
  const char* xa = (const char*)d_in[0];
  const char* wqkv = (const char*)d_in[1];
  const char* wo = (const char*)d_in[2];
  const char* xb = (const char*)d_in[3];
  const char* cre = (const char*)d_in[4];
  const char* cim = (const char*)d_in[5];
  const char* dre = (const char*)d_in[6];
  const char* dim = (const char*)d_in[7];
  char* ws = (char*)d_ws;
  hipFuncSetAttribute((const void*)k1_proj, hipFuncAttributeMaxDynamicSharedMemorySize, K1_LDS);
  hipFuncSetAttribute((const void*)k2_attn, hipFuncAttributeMaxDynamicSharedMemorySize, K2_LDS);
  hipFuncSetAttribute((const void*)k3_out, hipFuncAttributeMaxDynamicSharedMemorySize, K3_LDS);
  k1_proj<<<256, 512, K1_LDS, stream>>>(xa, wqkv, xb, dre, dim, ws);
  k2_attn<<<256, K2_THREADS, K2_LDS, stream>>>(cre, cim, ws);
  k3_out<<<256, 512, K3_LDS, stream>>>(wo, ws, (char*)d_out);
}
```

```cpp
#define OUT_AUX 17
#include <hip/hip_runtime.h>
#include <hip/hip_bf16.h>
#include <stdint.h>

typedef __attribute__((ext_vector_type(8))) short bf16x8;
typedef __attribute__((ext_vector_type(4))) short s16x4;
typedef __attribute__((ext_vector_type(16))) float f32x16;
typedef __attribute__((ext_vector_type(4))) unsigned u32x4;
typedef __attribute__((ext_vector_type(2))) unsigned u32x2;
typedef __attribute__((ext_vector_type(4))) float f32x4;
#define LAS __attribute__((address_space(3)))
typedef LAS const char* lds_cptr;

#define SBAR() __builtin_amdgcn_sched_barrier(0)
#define PIN(x) asm volatile("" : "+v"(x))
#define MFMA(a, b, c) __builtin_amdgcn_mfma_f32_32x32x16_bf16(a, b, c, 0, 0, 0)

constexpr int S = 2048, DM = 512, NH = 32;
constexpr size_t WS_Q = 0;
constexpr size_t WS_K = 2u << 20;
constexpr size_t WS_V = 4u << 20;
constexpr size_t WS_AB = 6u << 20;
constexpr size_t WS_O = 10u << 20;
constexpr size_t WS_P = 12u << 20;
#define STEPSTAMP(n)
#define STAMP(kid, i)
#define STAMPC(kid, i)

typedef __attribute__((ext_vector_type(2))) float f32x2;
typedef __attribute__((ext_vector_type(2))) __bf16 bf16x2;
__device__ __forceinline__ unsigned cvtpk(float lo, float hi) { const f32x2 v = {lo, hi}; return __builtin_bit_cast(unsigned, __builtin_convertvector(v, bf16x2)); }
__device__ __forceinline__ void dma16(const void* sbase, unsigned voff, unsigned ldsaddr) {
  asm volatile("s_mov_b32 m0, %2\n\ts_nop 0\n\tglobal_load_lds_dwordx4 %0, %1" ::"v"(voff), "s"(sbase), "s"(ldsaddr) : "memory");
}
#define WAIT_BAR(N) asm volatile("s_waitcnt vmcnt(" #N ") lgkmcnt(0)\n\ts_barrier" ::: "memory")
__device__ __forceinline__ bf16x8 ldsfrag(lds_cptr p) { return *(LAS const bf16x8*)p; }
typedef short v4i16_t __attribute__((ext_vector_type(4)));
__device__ __forceinline__ s16x4 ldstr(lds_cptr p) { return __builtin_bit_cast(s16x4, __builtin_amdgcn_ds_read_tr16_b64_v4i16((LAS v4i16_t*)p)); }
__device__ __forceinline__ bf16x8 cat8(s16x4 a, s16x4 b) { return (bf16x8){a[0], a[1], a[2], a[3], b[0], b[1], b[2], b[3]}; }
__device__ __forceinline__ u32x4 pack8(const f32x16& c, int r0, float sc) {
  return (u32x4){cvtpk(c[r0] * sc, c[r0 + 1] * sc), cvtpk(c[r0 + 2] * sc, c[r0 + 3] * sc), cvtpk(c[r0 + 4] * sc, c[r0 + 5] * sc), cvtpk(c[r0 + 6] * sc, c[r0 + 7] * sc)};
}
__device__ __forceinline__ u32x4 pack8(const f32x16& c, int r0) {
  return (u32x4){cvtpk(c[r0], c[r0 + 1]), cvtpk(c[r0 + 2], c[r0 + 3]), cvtpk(c[r0 + 4], c[r0 + 5]), cvtpk(c[r0 + 6], c[r0 + 7])};
}
__device__ __forceinline__ u32x4 natorder(u32x4 w) {
  auto a = __builtin_amdgcn_permlane32_swap(w[0], w[2], false, false);
  auto b = __builtin_amdgcn_permlane32_swap(w[1], w[3], false, false);
  return (u32x4){a[0], b[0], a[1], b[1]};
}

#define WS_T0 (40u << 20)
#define PROBE_SET()
#define PROBE_WAIT(T)
constexpr int K1_SLOT = 40960, K1_LDS = 4 * K1_SLOT;
#define RA 2
#define ST_AUX 17
__device__ __forceinline__ void st16(__amdgpu_buffer_rsrc_t r, unsigned off, u32x4 v) { __builtin_amdgcn_raw_buffer_store_b128(v, r, off, 0, ST_AUX); }
__device__ __forceinline__ __amdgpu_buffer_rsrc_t mkrsrc(const void* p, unsigned bytes) { return __builtin_amdgcn_make_buffer_rsrc((void*)p, (short)0, (int)bytes, 0x00020000); }
#define WAITV(N) asm volatile("s_waitcnt vmcnt(" #N ")" ::: "memory")
#define WAITL() asm volatile("s_waitcnt lgkmcnt(0)" ::: "memory")
#define BAR() asm volatile("s_barrier" ::: "memory")

#define dma16k1 dma16
__global__ void __launch_bounds__(512, 2) k1_proj(const char* __restrict__ xa, const char* __restrict__ wqkv, const char* __restrict__ xb,
                                                  const char* __restrict__ dre, const char* __restrict__ dim, char* __restrict__ ws) {
  extern __shared__ __attribute__((aligned(16))) char lds[];
  const int lane = threadIdx.x & 63, w = __builtin_amdgcn_readfirstlane(threadIdx.x >> 6), hi = lane >> 5;
  const int x = blockIdx.x & 7, k = blockIdx.x >> 3;
  const int rb = (x & 3) * 4 + (k & 3), cb = (x >> 2) * 4 + ((k >> 2) & 3);
  const unsigned lds0 = (unsigned)(uintptr_t)lds;
  const bool qkv = k < 16;
  PROBE_SET();
  STAMP(0, 0);
  if (w >= 4) {
    const int p = w - 4;
    const int rot = (k + x) & 7;
    unsigned vx[2];
#pragma unroll
    for (int j = 0; j < 2; ++j) { const int row = 8 * j + (lane >> 3); vx[j] = row * 1024 + (((lane & 7) ^ ((row >> 1) & 7)) << 4); }
    if (qkv) {
      const char* xs = xa + (size_t)(rb * 128 + p * 32) * 1024;
      const char* wsrc = wqkv + cb * 128 + (size_t)p * 16 * 3072;
      const int wr = lane >> 3;
      const unsigned vw = wr * 3072 + (((lane & 7) ^ (((wr >> 1) & 1) << 2)) << 4);
      auto issue = [&](int s) {
        const int ks = (s + rot) & 7;
        const unsigned sl = lds0 + (s & 3) * K1_SLOT;
        const char* xp = xs + ks * 128;
        const char* wp = wsrc + (size_t)ks * 64 * 3072;
#pragma unroll
        for (int i = 0; i < 4; ++i) dma16k1(xp + (i >> 1) * 16384, vx[i & 1], sl + p * 4096 + i * 1024);
#pragma unroll
        for (int i = 0; i < 6; ++i) dma16k1(wp + (i >> 1) * 1024 + (i & 1) * 8 * 3072, vw, sl + 16384 + (i >> 1) * 8192 + p * 2048 + (i & 1) * 1024);
      };
      issue(0); issue(1);
      WAITV(10); BAR();
      issue(2);
      for (int s = 1; s < 8; ++s) {
        if (s < 7) WAITV(10); else WAITV(0);
        BAR();
        if (s + RA < 8) issue(s + RA);
      }
    } else {
      const char* xs = xb + (size_t)(rb * 128 + p * 32) * 1024;
      const char* ds = ((cb >> 2) ? dim : dre) + (size_t)((cb & 3) * 128 + p * 32) * 1024;
      auto issue = [&](int s) {
        const int ks = (s + rot) & 7;
        const unsigned sl = lds0 + (s & 3) * K1_SLOT;
#pragma unroll
        for (int i = 0; i < 4; ++i) dma16k1(xs + ks * 128 + (i >> 1) * 16384, vx[i & 1], sl + p * 4096 + i * 1024);
#pragma unroll
        for (int i = 0; i < 4; ++i) dma16k1(ds + ks * 128 + (i >> 1) * 16384, vx[i & 1], sl + 16384 + p * 4096 + i * 1024);
      };
      issue(0); issue(1);
      WAITV(8); BAR();
      issue(2);
      for (int s = 1; s < 8; ++s) {
        if (s < 7) WAITV(8); else WAITV(0);
        BAR();
        if (s + RA < 8) issue(s + RA);
      }
    }
    return;
  }
  const __amdgpu_buffer_rsrc_t wsr = mkrsrc(ws, WS_P);
  const int tm = w & 1, ch = w >> 1;
  const int frow = 64 * tm + (lane & 31);
  const unsigned xa0 = lds0 + frow * 128 + ((hi ^ ((frow >> 1) & 7)) << 4);
  auto ldx = [&](unsigned so, int ks, int t) { return ldsfrag((lds_cptr)(uintptr_t)((xa0 ^ (ks << 5)) + so) + t * 4096); };
  if (qkv) {
    const int q4 = (lane & 15) >> 2, p4 = lane & 3, blk = (lane >> 4) & 1;
    const unsigned wa0 = lds0 + 16384 + (8 * hi + q4) * 128 + (((4 * ch + 2 * blk + (p4 >> 1)) ^ ((q4 >> 1) << 2)) << 4) + (p4 & 1) * 8;
    auto ldw = [&](unsigned so, int ks, int m) { const lds_cptr wp = (lds_cptr)(uintptr_t)(wa0 + so) + ks * 2048 + m * 8192; return cat8(ldstr(wp), ldstr(wp + 512)); };
    f32x16 aq0 = {}, aq1 = {}, ak0 = {}, ak1 = {}, av0 = {}, av1 = {};
    bf16x8 x0, x1, wq, wk, wv, nx0, nx1, nwq, nwk, nwv;
    BAR();
    STAMP(0, 8); STAMPC(0, 24);
    x0 = ldx(0, 0, 0); wq = ldw(0, 0, 0); x1 = ldx(0, 0, 1); wk = ldw(0, 0, 1); wv = ldw(0, 0, 2);
#define QSUB(so, ks, SYNC, LOAD) do { \
      if (SYNC) WAITL(); SBAR(); \
      aq0 = MFMA(wq, x0, aq0); SBAR(); \
      if (SYNC) BAR(); SBAR(); \
      if (LOAD) { nx0 = ldx(so, ks, 0); nwq = ldw(so, ks, 0); } SBAR(); \
      aq1 = MFMA(wq, x1, aq1); SBAR(); \
      if (LOAD) { nx1 = ldx(so, ks, 1); nwk = ldw(so, ks, 1); } SBAR(); \
      ak0 = MFMA(wk, x0, ak0); SBAR(); \
      if (LOAD) { nwv = ldw(so, ks, 2); } SBAR(); \
      ak1 = MFMA(wk, x1, ak1); SBAR(); \
      av0 = MFMA(x0, wv, av0); SBAR(); \
      av1 = MFMA(x1, wv, av1); SBAR(); \
      if (LOAD) { x0 = nx0; x1 = nx1; wq = nwq; wk = nwk; wv = nwv; } \
    } while (0)
#pragma unroll
    for (int s = 0; s < 8; ++s) {
      const unsigned so = (s & 3) * K1_SLOT, sn = ((s + 1) & 3) * K1_SLOT;
      QSUB(so, 1, false, true); QSUB(so, 2, false, true); QSUB(so, 3, false, true);
      QSUB(sn, 0, s < 7, s < 7);
    }
    STAMP(0, 1); STAMPC(0, 25);
    constexpr float QS = 0.25f * 1.4426950408889634f;
    const int h0 = 4 * cb + 2 * ch, tile = rb * 4 + 2 * tm;
    const unsigned qo = WS_Q + ((h0 * 64 + tile) << 10) + lane * 16;
    st16(wsr, qo, pack8(aq0, 0, QS)); st16(wsr, qo + 65536, pack8(aq0, 8, QS));
    st16(wsr, qo + 1024, pack8(aq1, 0, QS)); st16(wsr, qo + 1024 + 65536, pack8(aq1, 8, QS));
    const unsigned ko = qo + (unsigned)(WS_K - WS_Q);
    st16(wsr, ko, pack8(ak0, 0)); st16(wsr, ko + 65536, pack8(ak0, 8));
    st16(wsr, ko + 1024, pack8(ak1, 0)); st16(wsr, ko + 1024 + 65536, pack8(ak1, 8));
    const unsigned vo = WS_V + ((h0 + ((lane >> 4) & 1)) << 16) + tile * 1024 + (hi * 16 + (lane & 15)) * 16;
    st16(wsr, vo, pack8(av0, 0)); st16(wsr, vo + 512, pack8(av0, 8));
    st16(wsr, vo + 1024, pack8(av1, 0)); st16(wsr, vo + 1024 + 512, pack8(av1, 8));
  } else {
    const int er = 64 * ch + (lane & 31);
    const unsigned da0 = lds0 + 16384 + er * 128 + ((hi ^ ((er >> 1) & 7)) << 4);
    auto ldd = [&](unsigned so, int ks, int t) { return ldsfrag((lds_cptr)(uintptr_t)((da0 ^ (ks << 5)) + so) + t * 4096); };
    f32x16 a00 = {}, a01 = {}, a10 = {}, a11 = {};
    bf16x8 x0, x1, d0, d1, nx0, nx1, nd0, nd1;
    BAR();
    STAMP(0, 17);
    x0 = ldx(0, 0, 0); d0 = ldd(0, 0, 0); d1 = ldd(0, 0, 1); x1 = ldx(0, 0, 1);
#define DSUB(so, ks, SYNC, LOAD) do { \
      if (SYNC) WAITL(); SBAR(); \
      a00 = MFMA(x0, d0, a00); SBAR(); \
      if (SYNC) BAR(); SBAR(); \
      if (LOAD) { nx0 = ldx(so, ks, 0); nd0 = ldd(so, ks, 0); } SBAR(); \
      a01 = MFMA(x0, d1, a01); SBAR(); \
      if (LOAD) { nd1 = ldd(so, ks, 1); nx1 = ldx(so, ks, 1); } SBAR(); \
      a10 = MFMA(x1, d0, a10); SBAR(); \
      a11 = MFMA(x1, d1, a11); SBAR(); \
      if (LOAD) { x0 = nx0; x1 = nx1; d0 = nd0; d1 = nd1; } \
    } while (0)
#pragma unroll
    for (int s = 0; s < 8; ++s) {
      const unsigned so = (s & 3) * K1_SLOT, sn = ((s + 1) & 3) * K1_SLOT;
      DSUB(so, 1, false, true); DSUB(so, 2, false, true); DSUB(so, 3, false, true);
      DSUB(sn, 0, s < 7, s < 7);
    }
    STAMP(0, 3);
    const int part = cb >> 2;
    const float sg = part ? -1.f : 1.f;
    const int et = (cb & 3) * 4 + 2 * ch, s16 = rb * 8 + tm * 4;
    const unsigned op = WS_AB + (((part * 128 + s16) * 16 + et) << 10) + lane * 16;
    st16(wsr, op, natorder(pack8(a00, 0, sg))); st16(wsr, op + 16384, natorder(pack8(a00, 8, sg)));
    st16(wsr, op + 1024, natorder(pack8(a01, 0, sg))); st16(wsr, op + 1024 + 16384, natorder(pack8(a01, 8, sg)));
    st16(wsr, op + 32768, natorder(pack8(a10, 0, sg))); st16(wsr, op + 32768 + 16384, natorder(pack8(a10, 8, sg)));
    st16(wsr, op + 32768 + 1024, natorder(pack8(a11, 0, sg))); st16(wsr, op + 32768 + 1024 + 16384, natorder(pack8(a11, 8, sg)));
  }
  STAMP(0, 2);
}

constexpr int K2_KR = 0, K2_VR = 16384, K2_CONST = 32768, K2_C = 36864, K2_B = K2_C + 4 * 16384, K2_LDS = K2_B + 3 * 16384;
#define K2_GEMM 1
constexpr int K2_THREADS = K2_GEMM ? 768 : 512;

__device__ __forceinline__ void attn_exact(const char* ws, int h, int qt, char* optr, int lane) {
  const int hi = lane >> 5;
  const bf16x8 qf = *(const bf16x8*)(ws + WS_Q + ((size_t)(h * 64 + qt) << 10) + lane * 16);
  f32x16 o = {};
  float m = -INFINITY;
  const bool isv = (lane & 31) < 16;
  const short one = ((lane & 31) == 16 || (lane & 31) == 20) ? (short)0x3f80 : (short)0;
  const bf16x8 cst = {one, one, one, one, one, one, one, one};
  for (int t = 0; t < 64; ++t) {
    const bf16x8 kf = *(const bf16x8*)(ws + WS_K + ((size_t)(h * 64 + t) << 10) + lane * 16);
    const char* vp = ws + WS_V + ((size_t)h << 16) + t * 1024 + (hi * 16 + (lane & 15)) * 16;
    bf16x8 v0 = *(const bf16x8*)vp, v1 = *(const bf16x8*)(vp + 512);
    v0 = isv ? v0 : cst; v1 = isv ? v1 : cst;
    f32x16 s = {};
    s = MFMA(kf, qf, s);
    float mx = s[0];
#pragma unroll
    for (int r = 1; r < 16; ++r) mx = fmaxf(mx, s[r]);
    mx = fmaxf(mx, __shfl_xor(mx, 32));
    const float mn = fmaxf(m, mx), al = __builtin_amdgcn_exp2f(m - mn);
    m = mn;
#pragma unroll
    for (int r = 0; r < 16; ++r) { o[r] *= al; s[r] = __builtin_amdgcn_exp2f(s[r] - mn); }
    o = MFMA(v0, __builtin_bit_cast(bf16x8, pack8(s, 0)), o);
    o = MFMA(v1, __builtin_bit_cast(bf16x8, pack8(s, 8)), o);
  }
  *(u32x4*)optr = pack8(o, 0, 1.f / o[8]);
}

__global__ void __launch_bounds__(K2_THREADS, K2_GEMM ? 3 : 2) k2_attn(const char* __restrict__ cre, const char* __restrict__ cim, char* __restrict__ ws) {
  extern __shared__ __attribute__((aligned(16))) char lds[];
  const int lane = threadIdx.x & 63, w = __builtin_amdgcn_readfirstlane(threadIdx.x >> 6), hi = lane >> 5;
  const int x = blockIdx.x & 7, k = blockIdx.x >> 3;
  const unsigned lds0 = (unsigned)(uintptr_t)lds;
  STAMP(1, 0);
  if (w < 8) {
    const int h = 4 * x + (k >> 3), qt = (k & 7) * 8 + w;
    const char* src = ws + (w < 4 ? WS_K : WS_V) + ((size_t)h << 16) + (w & 3) * 1024;
    const unsigned dst = lds0 + (w < 4 ? K2_KR : K2_VR) + (w & 3) * 1024;
    const int ahead = w < 4 ? 3 : 2;
    const unsigned vl = lane * 16;
    for (int i = 0; i < ahead; ++i) dma16(src + i * 4096, vl, dst + i * 4096);
    if (threadIdx.x < 256) {
      const int c = threadIdx.x;
      const unsigned one = ((c & 31) == 0 || (c & 31) == 4) ? 0x3f803f80u : 0u;
      *(LAS u32x4*)((LAS char*)lds + K2_CONST + c * 16) = (u32x4){one, one, one, one};
    }
    const bf16x8 qf = *(const bf16x8*)(ws + WS_Q + ((size_t)(h * 64 + qt) << 10) + lane * 16);
    const int r32 = lane & 31;
    const int cchunk = r32 < 20 ? r32 - 16 : (r32 < 28 ? (r32 == 20 ? 4 : 5) : 1);
    const unsigned vbase = r32 < 16 ? (unsigned)(K2_VR + (hi * 16 + r32) * 16) : (unsigned)(K2_CONST + cchunk * 16);
    const unsigned vstep = r32 < 16 ? 4096u : 0u;
    const lds_cptr kp = (lds_cptr)lds + K2_KR + lane * 16;
    if (w < 4) WAIT_BAR(2); else WAIT_BAR(1);
    f32x16 cm, sA, sB, o = {};
    {
      f32x16 z = {};
      sA = MFMA(ldsfrag(kp), qf, z);
      float mx = sA[0];
#pragma unroll
      for (int r = 1; r < 16; ++r) mx = fmaxf(mx, sA[r]);
      auto sw = __builtin_amdgcn_permlane32_swap(__float_as_uint(mx), __float_as_uint(mx), false, false);
      mx = fmaxf(__uint_as_float(sw[0]), __uint_as_float(sw[1]));
      const float nm = -(mx + 48.f);
#pragma unroll
      for (int r = 0; r < 16; ++r) { cm[r] = nm; sA[r] += nm; }
    }
    PIN(cm);
    u32x4 p0 = {}, p1 = {};
    bf16x8 kf = ldsfrag(kp + 1024), vf0, vf1;
    lds_cptr vp = (lds_cptr)lds + vbase;
    vf0 = ldsfrag(vp);
    vf1 = ldsfrag(vp + 512);
    if (w < 4) WAIT_BAR(1); else WAIT_BAR(0);
#define EX(v) __builtin_amdgcn_exp2f(v)
#define PVM(A, B, C) MFMA(A, B, C)
#define SM(A, B, C, OLD) MFMA(A, B, C)
#define LDF(P, OLD) ldsfrag(P)
#define TILE(CUR, NXT, KNEXT, V0NEXT, V1CUR, LASTT) do { \
      CUR[0] = EX(CUR[0]); CUR[1] = EX(CUR[1]); CUR[2] = EX(CUR[2]); CUR[3] = EX(CUR[3]); \
      p0[0] = cvtpk(CUR[0], CUR[1]); p0[1] = cvtpk(CUR[2], CUR[3]); PIN(p0); SBAR(); \
      o = PVM(vf1, __builtin_bit_cast(bf16x8, p1), o); SBAR(); \
      vf1 = LDF(V1CUR, vf1); SBAR(); \
      CUR[4] = EX(CUR[4]); CUR[5] = EX(CUR[5]); CUR[6] = EX(CUR[6]); CUR[7] = EX(CUR[7]); \
      p0[2] = cvtpk(CUR[4], CUR[5]); p0[3] = cvtpk(CUR[6], CUR[7]); PIN(p0); SBAR(); \
      NXT = SM(kf, qf, cm, NXT); SBAR(); \
      kf = LDF(KNEXT, kf); SBAR(); \
      CUR[8] = EX(CUR[8]); CUR[9] = EX(CUR[9]); CUR[10] = EX(CUR[10]); CUR[11] = EX(CUR[11]); \
      p1[0] = cvtpk(CUR[8], CUR[9]); p1[1] = cvtpk(CUR[10], CUR[11]); PIN(p1); SBAR(); \
      o = PVM(vf0, __builtin_bit_cast(bf16x8, p0), o); SBAR(); \
      if (!(LASTT)) { vf0 = LDF(V0NEXT, vf0); SBAR(); } \
      CUR[12] = EX(CUR[12]); CUR[13] = EX(CUR[13]); CUR[14] = EX(CUR[14]); CUR[15] = EX(CUR[15]); \
      p1[2] = cvtpk(CUR[12], CUR[13]); p1[3] = cvtpk(CUR[14], CUR[15]); PIN(p1); SBAR(); \
    } while (0)
#define STEP(n, sl, sl1, LAST) do { \
      STEPSTAMP(n); \
      if ((n) + ahead < 16) dma16(src + ((n) + ahead) * 4096, vl, dst + (((n) + ahead) & 3) * 4096); \
      const lds_cptr k0 = kp + (sl) * 4096, k1 = kp + (sl1) * 4096; \
      const lds_cptr v0 = (lds_cptr)lds + vbase + (sl) * vstep; \
      const lds_cptr v1 = (lds_cptr)lds + vbase + (sl1) * vstep; \
      TILE(sA, sB, k0 + 2048, v0 + 1024, v0 + 512, 0); \
      TILE(sB, sA, k0 + 3072, v0 + 2048, v0 + 1536, 0); \
      TILE(sA, sB, k1, v0 + 3072, v0 + 2560, 0); \
        \
      TILE(sB, sA, k1 + 1024, v0 + 3072, v0 + 3584, 1); \
      if ((n) < 13) WAIT_BAR(1); else WAIT_BAR(0); \
      vf0 = LDF(v1, vf0); \
    } while (0)
    STAMP(1, 3); STAMPC(1, 24);
    for (int n = 0; n < 16; n += 4) {
      STEP(n, 0, 1, 0); STEP(n + 1, 1, 2, 0); STEP(n + 2, 2, 3, 0); STEP(n + 3, 3, 0, 0);
    }
    o = MFMA(vf1, __builtin_bit_cast(bf16x8, p1), o);
    STAMP(1, 1); STAMPC(1, 25); STEPSTAMP(16);
    char* optr = ws + WS_O + ((size_t)(qt * 32 + h) << 10) + lane * 16;
    const float l = o[8];
    const bool bad = !(l > 0x1p-100f && l < 0x1p100f);
    if (__builtin_expect(__any(bad), 0)) attn_exact(ws, h, qt, optr, lane);
    else *(u32x4*)optr = pack8(o, 0, 1.f / l);
  }
  else {
    const int g = w - 8;
    const int ksp = x >> 1, tb = (x & 1) * 8 + (k >> 2), eb = k & 3;
#define GPRIO 2
#define GSLEEP 0
    __builtin_amdgcn_s_setprio(GPRIO);
    const char* src;
    unsigned voff[2];
    unsigned dst;
    size_t sstep;
    int ahead, nslot;
    if (g < 2) {
      src = ((ksp >> 1) ? cim : cre) + (size_t)(tb * 128 + g * 64) * 4096 + (ksp & 1) * 2048;
#pragma unroll
      for (int j = 0; j < 2; ++j) { const int row = 8 * j + (lane >> 3); voff[j] = row * 4096 + (((lane & 7) ^ ((row >> 1) & 7)) << 4); }
      dst = lds0 + K2_C + g * 8192; sstep = 128; ahead = 3; nslot = 4;
    } else {
      src = ws + WS_AB + ((size_t)(((ksp >> 1) * 128 + (ksp & 1) * 64 + 2 * (g - 2)) * 16 + eb * 4) << 10);
      voff[0] = lane * 16; voff[1] = lane * 16;
      dst = lds0 + K2_B + (g - 2) * 8192; sstep = 4 * 16 * 1024; ahead = 2; nslot = 3;
    }
    auto issue = [&](int n, int slot) {
      const char* p = src + (size_t)n * sstep;
      const unsigned d = dst + slot * 16384;
      if (g < 2) {
#pragma unroll
        for (int i = 0; i < 8; ++i) dma16(p + (size_t)(i >> 1) * 16 * 4096, voff[i & 1], d + i * 1024);
      } else {
#pragma unroll
        for (int i = 0; i < 8; ++i) dma16(p + (i >> 2) * 16384 + (i & 3) * 1024, voff[0], d + i * 1024);
      }
    };
    for (int i = 0; i < ahead; ++i) issue(i, i);
    const int tm = g & 1, en = g >> 1;
    const int tr0 = 64 * tm + (lane & 31), tr1 = tr0 + 32;
    const unsigned cf0 = K2_C + tr0 * 128 + ((hi ^ ((tr0 >> 1) & 7)) << 4), cf1 = K2_C + tr1 * 128 + ((hi ^ ((tr1 >> 1) & 7)) << 4);
    const unsigned bf = K2_B + en * 2048 + lane * 16;
    f32x16 a00 = {}, a01 = {}, a10 = {}, a11 = {};
    if (g < 2) WAIT_BAR(16); else WAIT_BAR(8);
    asm volatile("s_barrier" ::: "memory");
    int cs = 0, bs = 0;
    for (int n = 0; n < 16; ++n) {
      if (n + ahead < 16) issue(n + ahead, g < 2 ? ((cs + 3) & 3) : (bs == 0 ? 2 : bs - 1));
      const lds_cptr cb_ = (lds_cptr)lds + cs * 16384, bb_ = (lds_cptr)lds + bf + bs * 16384;
#pragma unroll
      for (int ks = 0; ks < 4; ++ks) {
        const bf16x8 c0 = ldsfrag(cb_ + (cf0 ^ (ks << 5))), c1 = ldsfrag(cb_ + (cf1 ^ (ks << 5)));
        const bf16x8 b0 = ldsfrag(bb_ + ks * 4096), b1 = ldsfrag(bb_ + ks * 4096 + 1024);
        a00 = MFMA(b0, c0, a00); a01 = MFMA(b0, c1, a01);
        a10 = MFMA(b1, c0, a10); a11 = MFMA(b1, c1, a11);
      }
      if (g < 2) { if (n < 13) WAIT_BAR(16); else if (n == 13) WAIT_BAR(8); else WAIT_BAR(0); }
      else { if (n < 14) WAIT_BAR(8); else WAIT_BAR(0); }
      cs = (cs + 1) & 3; bs = bs == 2 ? 0 : bs + 1;
    }
    const int tt = tb * 4 + 2 * tm, et = eb * 4 + 2 * en;
    char* pp = ws + WS_P + ((size_t)((ksp * 64 + tt) * 16 + et) << 11) + lane * 16;
    *(u32x4*)(pp) = pack8(a00, 0); *(u32x4*)(pp + 1024) = pack8(a00, 8);
    *(u32x4*)(pp + 2048) = pack8(a10, 0); *(u32x4*)(pp + 2048 + 1024) = pack8(a10, 8);
    *(u32x4*)(pp + 32768) = pack8(a01, 0); *(u32x4*)(pp + 32768 + 1024) = pack8(a01, 8);
    *(u32x4*)(pp + 32768 + 2048) = pack8(a11, 0); *(u32x4*)(pp + 32768 + 2048 + 1024) = pack8(a11, 8);
    STAMP(1, 1);
  }
  STAMP(1, 2);
}

constexpr int K3_LDS = 65536;
__global__ void __launch_bounds__(512, 2) k3_out(const char* __restrict__ wo, const char* __restrict__ ws_, char* __restrict__ out) {
  extern __shared__ __attribute__((aligned(16))) char lds[];
  char* ws = (char*)ws_;
  const int lane = threadIdx.x & 63, w = __builtin_amdgcn_readfirstlane(threadIdx.x >> 6), hi = lane >> 5;
  const int x = blockIdx.x & 7, k = blockIdx.x >> 3;
  const unsigned lds0 = (unsigned)(uintptr_t)lds;
  STAMP(2, 0);
  const int tb = 4 * x + (k >> 3), nb = k & 7;
  {
    const int r = lane >> 3;
    const unsigned vo = r * 1024 + (((lane & 7) ^ (((r >> 1) & 1) << 2)) << 4);
#pragma unroll
    for (int i = 0; i < 8; ++i) dma16(wo + nb * 128 + (size_t)(w * 8 + i) * 8 * 1024, vo, lds0 + (w * 8 + i) * 1024);
  }
  const int kh = w >> 2, tm = w & 1, nn = (w >> 1) & 1;
  const int tt = tb * 2 + tm;
  bf16x8 of[16];
#pragma unroll
  for (int i = 0; i < 16; ++i) of[i] = *(const bf16x8*)(ws + WS_O + ((size_t)(tt * 32 + kh * 16 + i) << 10) + lane * 16);
  f32x16 ys;
  {
    const int et = nb * 2 + nn;
    const char* pp = ws + WS_P + ((size_t)((2 * kh * 64 + tt) * 16 + et) << 11) + lane * 16;
    const u32x4 a0 = *(const u32x4*)pp, a1 = *(const u32x4*)(pp + 1024);
    const u32x4 b0 = *(const u32x4*)(pp + (64 * 16 << 11)), b1 = *(const u32x4*)(pp + (64 * 16 << 11) + 1024);
#pragma unroll
    for (int i = 0; i < 4; ++i) {
      ys[2 * i] = __uint_as_float(a0[i] << 16) + __uint_as_float(b0[i] << 16);
      ys[2 * i + 1] = __uint_as_float(a0[i] & 0xffff0000u) + __uint_as_float(b0[i] & 0xffff0000u);
      ys[8 + 2 * i] = __uint_as_float(a1[i] << 16) + __uint_as_float(b1[i] << 16);
      ys[8 + 2 * i + 1] = __uint_as_float(a1[i] & 0xffff0000u) + __uint_as_float(b1[i] & 0xffff0000u);
    }
  }
  asm volatile("s_waitcnt vmcnt(0)\n\ts_barrier" ::: "memory");
  const int q4 = (lane & 15) >> 2, p4 = lane & 3, blk = (lane >> 4) & 1;
  const lds_cptr wf = (lds_cptr)lds + (kh * 256 + 4 * hi + q4) * 128 + (((4 * nn + 2 * blk + (p4 >> 1)) ^ ((q4 >> 1) << 2)) << 4) + (p4 & 1) * 8;
  f32x16 acc = {};
#pragma unroll
  for (int i = 0; i < 16; ++i) {
    const bf16x8 a = cat8(ldstr(wf + i * 2048), ldstr(wf + i * 2048 + 1024));
    acc = MFMA(a, of[i], acc);
  }
  __syncthreads();
  if (kh == 1) {
    LAS float* r = (LAS float*)((LAS char*)lds + (w & 3) * 8192);
#pragma unroll
    for (int i = 0; i < 4; ++i) {
      *(LAS f32x4*)(r + i * 256 + lane * 4) = (f32x4){acc[4 * i], acc[4 * i + 1], acc[4 * i + 2], acc[4 * i + 3]};
      *(LAS f32x4*)(r + 1024 + i * 256 + lane * 4) = (f32x4){ys[4 * i], ys[4 * i + 1], ys[4 * i + 2], ys[4 * i + 3]};
    }
  }
  __syncthreads();
  if (kh == 0) {
    const LAS float* r = (const LAS float*)((LAS char*)lds + (w & 3) * 8192);
#pragma unroll
    for (int i = 0; i < 4; ++i) {
      const f32x4 u = *(const LAS f32x4*)(r + i * 256 + lane * 4);
      const f32x4 v = *(const LAS f32x4*)(r + 1024 + i * 256 + lane * 4);
#pragma unroll
      for (int j = 0; j < 4; ++j) { acc[4 * i + j] += u[j]; ys[4 * i + j] += v[j]; }
    }
    const int row = tt * 32 + (lane & 31);
    const __amdgpu_buffer_rsrc_t outr = mkrsrc(out, 2048 * 2048);
    const unsigned op = row * 2048 + (nb * 64 + nn * 32 + hi * 8) * 2;
    __builtin_amdgcn_raw_buffer_store_b128(natorder(pack8(acc, 0)), outr, op, 0, OUT_AUX);
    __builtin_amdgcn_raw_buffer_store_b128(natorder(pack8(acc, 8)), outr, op + 32, 0, OUT_AUX);
    __builtin_amdgcn_raw_buffer_store_b128(natorder(pack8(ys, 0)), outr, op + 1024, 0, OUT_AUX);
    __builtin_amdgcn_raw_buffer_store_b128(natorder(pack8(ys, 8)), outr, op + 1024 + 32, 0, OUT_AUX);
  }
  STAMP(2, 2);
}

extern "C" void kernel_launch(void* const* d_in, const int* in_sizes, int n_in, void* d_out, int out_size, void* d_ws, size_t ws_size, hipStream_t stream) {
  const char* xa = (const char*)d_in[0];
  const char* wqkv = (const char*)d_in[1];
  const char* wo = (const char*)d_in[2];
  const char* xb = (const char*)d_in[3];
  const char* cre = (const char*)d_in[4];
  const char* cim = (const char*)d_in[5];
  const char* dre = (const char*)d_in[6];
  const char* dim = (const char*)d_in[7];
  char* ws = (char*)d_ws;
  hipFuncSetAttribute((const void*)k1_proj, hipFuncAttributeMaxDynamicSharedMemorySize, K1_LDS);
  hipFuncSetAttribute((const void*)k2_attn, hipFuncAttributeMaxDynamicSharedMemorySize, K2_LDS);
  hipFuncSetAttribute((const void*)k3_out, hipFuncAttributeMaxDynamicSharedMemorySize, K3_LDS);
  k1_proj<<<256, 512, K1_LDS, stream>>>(xa, wqkv, xb, dre, dim, ws);
  k2_attn<<<256, K2_THREADS, K2_LDS, stream>>>(cre, cim, ws);
  k3_out<<<256, 512, K3_LDS, stream>>>(wo, ws, (char*)d_out);
}
```

```cpp
#include <hip/hip_runtime.h>
#include <hip/hip_bf16.h>
#include <stdint.h>

typedef __attribute__((ext_vector_type(8))) short bf16x8;
typedef __attribute__((ext_vector_type(4))) short s16x4;
typedef __attribute__((ext_vector_type(16))) float f32x16;
typedef __attribute__((ext_vector_type(4))) unsigned u32x4;
typedef __attribute__((ext_vector_type(4))) float f32x4;
#define LAS __attribute__((address_space(3)))
typedef LAS const char* lds_cptr;

#define SBAR() __builtin_amdgcn_sched_barrier(0)
#define PIN(x) asm volatile("" : "+v"(x))
#define MFMA(a, b, c) __builtin_amdgcn_mfma_f32_32x32x16_bf16(a, b, c, 0, 0, 0)

constexpr size_t WS_Q = 0;
constexpr size_t WS_K = 2u << 20;
constexpr size_t WS_V = 4u << 20;
constexpr size_t WS_AB = 6u << 20;
constexpr size_t WS_O = 10u << 20;
constexpr size_t WS_P = 12u << 20;
constexpr unsigned WS_END = 20u << 20;
constexpr size_t WS_F = 20u << 20;
typedef __attribute__((address_space(1))) unsigned gu32;

typedef __attribute__((ext_vector_type(2))) float f32x2;
typedef __attribute__((ext_vector_type(2))) __bf16 bf16x2;
__device__ __forceinline__ unsigned cvtpk(float lo, float hi) { const f32x2 v = {lo, hi}; return __builtin_bit_cast(unsigned, __builtin_convertvector(v, bf16x2)); }
__device__ __forceinline__ void dma16(const void* sbase, unsigned voff, unsigned ldsaddr) {
  asm volatile("s_mov_b32 m0, %2\n\ts_nop 0\n\tglobal_load_lds_dwordx4 %0, %1" ::"v"(voff), "s"(sbase), "s"(ldsaddr) : "memory");
}
__device__ __forceinline__ void dma16_if(int on, const void* sbase, unsigned voff, unsigned ldsaddr) {
  asm volatile("s_cmp_lt_i32 %3, 1\n\ts_cbranch_scc1 1f\n\ts_mov_b32 m0, %2\n\ts_nop 0\n\tglobal_load_lds_dwordx4 %0, %1\n1:" ::"v"(voff), "s"(sbase), "s"(ldsaddr), "s"(on) : "scc", "memory");
}
#define WAIT_BAR(N) asm volatile("s_waitcnt vmcnt(" #N ") lgkmcnt(0)\n\ts_barrier" ::: "memory")
__device__ __forceinline__ bf16x8 ldsfrag(lds_cptr p) { return *(LAS const bf16x8*)p; }
typedef short v4i16_t __attribute__((ext_vector_type(4)));
__device__ __forceinline__ s16x4 ldstr(lds_cptr p) { return __builtin_bit_cast(s16x4, __builtin_amdgcn_ds_read_tr16_b64_v4i16((LAS v4i16_t*)p)); }
__device__ __forceinline__ bf16x8 cat8(s16x4 a, s16x4 b) { return (bf16x8){a[0], a[1], a[2], a[3], b[0], b[1], b[2], b[3]}; }
__device__ __forceinline__ u32x4 pack8(const f32x16& c, int r0, float sc) {
  return (u32x4){cvtpk(c[r0] * sc, c[r0 + 1] * sc), cvtpk(c[r0 + 2] * sc, c[r0 + 3] * sc), cvtpk(c[r0 + 4] * sc, c[r0 + 5] * sc), cvtpk(c[r0 + 6] * sc, c[r0 + 7] * sc)};
}
__device__ __forceinline__ u32x4 pack8(const f32x16& c, int r0) {
  return (u32x4){cvtpk(c[r0], c[r0 + 1]), cvtpk(c[r0 + 2], c[r0 + 3]), cvtpk(c[r0 + 4], c[r0 + 5]), cvtpk(c[r0 + 6], c[r0 + 7])};
}
__device__ __forceinline__ u32x4 natorder(u32x4 w) {
  auto a = __builtin_amdgcn_permlane32_swap(w[0], w[2], false, false);
  auto b = __builtin_amdgcn_permlane32_swap(w[1], w[3], false, false);
  return (u32x4){a[0], b[0], a[1], b[1]};
}

constexpr int WT_AUX = 17;
constexpr int DEV_AUX = 16;
__device__ __forceinline__ void st16(__amdgpu_buffer_rsrc_t r, unsigned off, u32x4 v) { __builtin_amdgcn_raw_buffer_store_b128(v, r, off, 0, WT_AUX); }
__device__ __forceinline__ __amdgpu_buffer_rsrc_t mkrsrc(const void* p, unsigned bytes) { return __builtin_amdgcn_make_buffer_rsrc((void*)p, (short)0, (int)bytes, 0x00020000); }
#define WAITV(N) asm volatile("s_waitcnt vmcnt(" #N ")" ::: "memory")
#define WAITL() asm volatile("s_waitcnt lgkmcnt(0)" ::: "memory")
#define BAR() asm volatile("s_barrier" ::: "memory")

constexpr int K2_KR = 0, K2_VR = 16384, K2_CONST = 32768, K2_C = 36864, K2_B = K2_C + 4 * 16384, K2_PF = K2_B + 3 * 16384, K2_FL = K2_PF + 2048, K2_LDS = K2_FL + 256;
constexpr int K2_THREADS = 768;
constexpr float MARGIN = 96.f;
constexpr int GPRIO = 3;
typedef LAS volatile unsigned* lds_flag;
__device__ __forceinline__ void flag_wait(unsigned f, lds_flag p, unsigned n) {
  if (__builtin_expect(__builtin_amdgcn_ballot_w64(f < n) != 0, 0)) {
    do { __builtin_amdgcn_s_sleep(1); f = *p; } while (__builtin_amdgcn_ballot_w64(f < n) != 0);
  }
}
__device__ __forceinline__ u32x4 attn_exact(const char* ws, int h, int qt, int lane) {
  const int hi = lane >> 5;
  const bf16x8 qf = *(const bf16x8*)(ws + WS_Q + ((size_t)(h * 64 + qt) << 10) + lane * 16);
  f32x16 o = {};
  float m = -INFINITY;
  const bool isv = (lane & 31) < 16;
  const short one = ((lane & 31) == 16 || (lane & 31) == 20) ? (short)0x3f80 : (short)0;
  const bf16x8 cst = {one, one, one, one, one, one, one, one};
  for (int t = 0; t < 64; ++t) {
    const bf16x8 kf = *(const bf16x8*)(ws + WS_K + ((size_t)(h * 64 + t) << 10) + lane * 16);
    const char* vp = ws + WS_V + ((size_t)h << 16) + t * 1024 + (hi * 16 + (lane & 15)) * 16;
    bf16x8 v0 = *(const bf16x8*)vp, v1 = *(const bf16x8*)(vp + 512);
    v0 = isv ? v0 : cst; v1 = isv ? v1 : cst;
    f32x16 s = {};
    s = MFMA(kf, qf, s);
    float mx = s[0];
#pragma unroll
    for (int r = 1; r < 16; ++r) mx = fmaxf(mx, s[r]);
    mx = fmaxf(mx, __shfl_xor(mx, 32));
    const float mn = fmaxf(m, mx), al = __builtin_amdgcn_exp2f(m - mn);
    m = mn;
#pragma unroll
    for (int r = 0; r < 16; ++r) { o[r] *= al; s[r] = __builtin_amdgcn_exp2f(s[r] - mn); }
    o = MFMA(v0, __builtin_bit_cast(bf16x8, pack8(s, 0)), o);
    o = MFMA(v1, __builtin_bit_cast(bf16x8, pack8(s, 8)), o);
  }
  return pack8(o, 0, 1.f / o[8]);
}

extern "C" __global__ void __launch_bounds__(K2_THREADS, 3) k2_attn(const char* __restrict__ cre, const char* __restrict__ cim, char* __restrict__ ws, const char* __restrict__ wo, char* __restrict__ out) {
  extern __shared__ __attribute__((aligned(16))) char lds[];
  const int lane = threadIdx.x & 63, w = __builtin_amdgcn_readfirstlane(threadIdx.x >> 6), hi = lane >> 5;
  const int x = blockIdx.x & 7, k = blockIdx.x >> 3;
  const unsigned lds0 = (unsigned)(uintptr_t)lds;
  if (__builtin_expect(w < 8, 1)) {
    const int h = 4 * x + (k >> 3), half = w >> 2, qt = (k & 7) * 8 + 2 * (w & 3);
    bf16x8 qa, qb;
    const unsigned vl = lane * 16;
    {
      const char* qp = ws + WS_Q + ((size_t)(h * 64 + qt) << 10);
      asm volatile("global_load_dwordx4 %0, %2, %3\n\tglobal_load_dwordx4 %1, %2, %3 offset:1024" : "=&v"(qa), "=&v"(qb) : "v"(vl), "s"(qp) : "memory");
    }
    const char* src = ws + (w < 4 ? WS_K : WS_V) + ((size_t)h << 16) + (w & 3) * 1024;
    const unsigned dst = lds0 + (w < 4 ? K2_KR : K2_VR) + (w & 3) * 1024;
    for (int i = 0; i < 3; ++i) dma16(src + i * 4096, vl, dst + i * 4096);
    if (threadIdx.x < 256) {
      const int c = threadIdx.x;
      const unsigned one = ((c & 31) == 0 || (c & 31) == 4) ? 0x3f803f80u : 0u;
      *(LAS u32x4*)((LAS char*)lds + K2_CONST + c * 16) = (u32x4){one, one, one, one};
    }
    if (threadIdx.x < 16) *(LAS unsigned*)((LAS char*)lds + K2_FL + threadIdx.x * 4) = 0;
    const int r32 = lane & 31;
    const int cchunk = r32 < 20 ? r32 - 16 : (r32 < 28 ? (r32 == 20 ? 4 : 5) : 1);
    const unsigned vbase = r32 < 16 ? (unsigned)(K2_VR + half * 2048 + (hi * 16 + r32) * 16) : (unsigned)(K2_CONST + cchunk * 16);
    const unsigned vstep = r32 < 16 ? 4096u : 0u;
    const lds_cptr kp = (lds_cptr)lds + K2_KR + half * 2048 + lane * 16;
    const lds_flag fown = (lds_flag)((LAS char*)lds + K2_FL + w * 4), fall = (lds_flag)((LAS char*)lds + K2_FL + (lane & 7) * 4);
    asm volatile("s_waitcnt lgkmcnt(0)\n\ts_barrier" ::: "memory");
    asm volatile("s_waitcnt vmcnt(2)" : "+v"(qa), "+v"(qb) :: "memory");
    *fown = 1;
    flag_wait(*fall, fall, 1);
    asm volatile("" ::: "memory");
    f32x16 cma, cmb, sA, sB, oA = {}, oB = {};
    {
      const f32x16 z = {};
      const bf16x8 k0 = ldsfrag((lds_cptr)lds + K2_KR + lane * 16);
      sA = MFMA(k0, qa, z);
      sB = MFMA(k0, qb, z);
      float ma = sA[0], mb = sB[0];
#pragma unroll
      for (int r = 1; r < 16; ++r) { ma = fmaxf(ma, sA[r]); mb = fmaxf(mb, sB[r]); }
      auto sa = __builtin_amdgcn_permlane32_swap(__float_as_uint(ma), __float_as_uint(ma), false, false);
      auto sb = __builtin_amdgcn_permlane32_swap(__float_as_uint(mb), __float_as_uint(mb), false, false);
      const float na = -(fmaxf(__uint_as_float(sa[0]), __uint_as_float(sa[1])) + MARGIN);
      const float nb = -(fmaxf(__uint_as_float(sb[0]), __uint_as_float(sb[1])) + MARGIN);
#pragma unroll
      for (int r = 0; r < 16; ++r) { cma[r] = na; cmb[r] = nb; }
    }
    PIN(cma); PIN(cmb);
    u32x4 p0 = {}, p1 = {};
    bf16x8 kf = ldsfrag(kp), vf0, vf1;
    const lds_cptr vp = (lds_cptr)lds + vbase;
    vf0 = ldsfrag(vp);
    vf1 = ldsfrag(vp + 512);
    sA = MFMA(kf, qa, cma);
#define APRIO(p) __builtin_amdgcn_s_setprio(p)
#define EX(v) __builtin_amdgcn_exp2f(v)
#define EXP4(C, r, P, i) do { C[r] = EX(C[r]); C[r + 1] = EX(C[r + 1]); C[r + 2] = EX(C[r + 2]); C[r + 3] = EX(C[r + 3]); \
      P[i] = cvtpk(C[r], C[r + 1]); P[i + 1] = cvtpk(C[r + 2], C[r + 3]); PIN(P); SBAR(); } while (0)
#define ASP(v) __builtin_bit_cast(bf16x8, v)
#define UNIT(CUR, NXT, OP, OC, QN, CN, A0, A1, A2, A3) do { \
      A0; SBAR(); \
      EXP4(CUR, 0, p0, 0); \
      OP = MFMA(vf1, ASP(p1), OP); SBAR(); \
      A1; SBAR(); \
      EXP4(CUR, 4, p0, 2); \
      NXT = MFMA(kf, QN, CN); SBAR(); \
      A2; SBAR(); \
      EXP4(CUR, 8, p1, 0); \
      OC = MFMA(vf0, ASP(p0), OC); SBAR(); \
      A3; SBAR(); \
      EXP4(CUR, 12, p1, 2); \
    } while (0)
#define STEP(n, sl, sl1) do { \
      const lds_cptr k0 = kp + (sl) * 4096, k1 = kp + (sl1) * 4096; \
      const lds_cptr v0 = (lds_cptr)lds + vbase + (sl) * vstep; \
      const lds_cptr v1 = (lds_cptr)lds + vbase + (sl1) * vstep; \
      unsigned fl; \
      UNIT(sA, sB, oB, oA, qb, cmb, \
           { APRIO(3); if ((n) < 14) asm volatile("s_waitcnt vmcnt(1)" ::: "memory"); else asm volatile("s_waitcnt vmcnt(0)" ::: "memory"); *fown = (n) + 2; }, \
           vf1 = ldsfrag(v0 + 512), kf = ldsfrag(k0 + 1024), {}); \
      UNIT(sB, sA, oA, oB, qa, cma, APRIO(2), {}, {}, { vf0 = ldsfrag(v0 + 1024); fl = *fall; }); \
      UNIT(sA, sB, oB, oA, qb, cmb, APRIO(1), vf1 = ldsfrag(v0 + 1024 + 512), \
           { flag_wait(fl, fall, (n) + 2); SBAR(); kf = ldsfrag(k1); if ((n) < 13) dma16(src + ((n) + 3) * 4096, vl, dst + (((n) + 3) & 3) * 4096); }, {}); \
      UNIT(sB, sA, oA, oB, qa, cma, APRIO(0), {}, {}, vf0 = ldsfrag(v1)); \
    } while (0)
    for (int n = 0; n < 16; n += 4) {
      STEP(n, 0, 1); STEP(n + 1, 1, 2); STEP(n + 2, 2, 3); STEP(n + 3, 3, 0);
    }
    oB = MFMA(vf1, ASP(p1), oB);
    {
      LAS char* mine = (LAS char*)lds + (half ? K2_VR : K2_KR) + (w & 3) * 2560 + lane * 16;
      const LAS char* theirs = (const LAS char*)lds + (half ? K2_KR : K2_VR) + (w & 3) * 2560 + lane * 16;
      f32x16 o;
      if (half) {
        *(LAS f32x4*)mine = (f32x4){oA[0], oA[1], oA[2], oA[3]}; *(LAS f32x4*)(mine + 1024) = (f32x4){oA[4], oA[5], oA[6], oA[7]};
        *(LAS float*)(mine + 2048 - lane * 12) = oA[8];
        o = oB;
      } else {
        *(LAS f32x4*)mine = (f32x4){oB[0], oB[1], oB[2], oB[3]}; *(LAS f32x4*)(mine + 1024) = (f32x4){oB[4], oB[5], oB[6], oB[7]};
        *(LAS float*)(mine + 2048 - lane * 12) = oB[8];
        o = oA;
      }
      asm volatile("" ::: "memory");
      *fown = 18;
      const lds_flag fp = (lds_flag)((LAS char*)lds + K2_FL + (w ^ 4) * 4);
      flag_wait(*fp, fp, 18);
      asm volatile("" ::: "memory");
      const f32x4 t0 = *(const LAS f32x4*)theirs, t1 = *(const LAS f32x4*)(theirs + 1024);
      const float tl = *(const LAS float*)(theirs + 2048 - lane * 12);
#pragma unroll
      for (int r = 0; r < 4; ++r) { o[r] += t0[r]; o[4 + r] += t1[r]; }
      const float l = o[8] + tl;
      const float om = fmaxf(fmaxf(fmaxf(fabsf(o[0]), fabsf(o[1])), fmaxf(fabsf(o[2]), fabsf(o[3]))), fmaxf(fmaxf(fabsf(o[4]), fabsf(o[5])), fmaxf(fabsf(o[6]), fabsf(o[7]))));
      const float osum = ((o[0] + o[1]) + (o[2] + o[3])) + ((o[4] + o[5]) + (o[6] + o[7]));
      const bool bad = !(l > 0x1p-112f && l < 0x1p100f) || !(om < 0x1p126f) || osum != osum;
      u32x4 res = pack8(o, 0, 1.f / l);
      if (__builtin_expect(__any(bad), 0)) res = attn_exact(ws, h, qt + half, lane);
      __builtin_amdgcn_raw_buffer_store_b128(res, mkrsrc(ws, WS_END), (unsigned)WS_O + (((qt + half) * 32 + h) << 10) + lane * 16, 0, WT_AUX);
    }
  } else {
    const int g = w - 8;
    const int ksp = x >> 1, tb = (x & 1) * 8 + (k >> 2), eb = k & 3;
    __builtin_amdgcn_s_setprio(GPRIO);
    const char* src;
    unsigned voff[2];
    unsigned dst;
    size_t sstep;
    int ahead;
    if (g < 2) {
      src = ((ksp >> 1) ? cim : cre) + (size_t)(tb * 128 + g * 64) * 4096 + (ksp & 1) * 2048;
#pragma unroll
      for (int j = 0; j < 2; ++j) { const int row = 8 * j + (lane >> 3); voff[j] = row * 4096 + (((lane & 7) ^ ((row >> 1) & 7)) << 4); }
      dst = lds0 + K2_C + g * 8192; sstep = 128; ahead = 3;
    } else {
      src = ws + WS_AB + ((size_t)(((ksp >> 1) * 128 + (ksp & 1) * 64 + 2 * (g - 2)) * 16 + eb * 4) << 10);
      voff[0] = lane * 16; voff[1] = lane * 16;
      dst = lds0 + K2_B + (g - 2) * 8192; sstep = 4 * 16 * 1024; ahead = 2;
    }
    auto issue = [&](int n, int slot) {
      const char* p = src + (size_t)n * sstep;
      const unsigned d = dst + slot * 16384;
      if (g < 2) {
#pragma unroll
        for (int i = 0; i < 8; ++i) dma16(p + (size_t)(i >> 1) * 16 * 4096, voff[i & 1], d + i * 1024);
      } else {
#pragma unroll
        for (int i = 0; i < 8; ++i) dma16(p + (i >> 2) * 16384 + (i & 3) * 1024, voff[0], d + i * 1024);
      }
    };
    asm volatile("s_barrier" ::: "memory");
    if (g >= 2) dma16(wo + blockIdx.x * 2048 + (g - 2) * 1024, lane * 16, lds0 + K2_PF + (g - 2) * 1024);
    for (int i = 0; i < ahead; ++i) issue(i, i);
    const int tm = g & 1, en = g >> 1;
    const int tr0 = 64 * tm + (lane & 31), tr1 = tr0 + 32;
    const unsigned cf0 = K2_C + tr0 * 128 + ((hi ^ ((tr0 >> 1) & 7)) << 4), cf1 = K2_C + tr1 * 128 + ((hi ^ ((tr1 >> 1) & 7)) << 4);
    const unsigned bf = K2_B + en * 2048 + lane * 16;
    const lds_flag fown = (lds_flag)((LAS char*)lds + K2_FL + 32 + g * 4), fall = (lds_flag)((LAS char*)lds + K2_FL + 32 + (lane & 3) * 4);
    f32x16 a00 = {}, a01 = {}, a10 = {}, a11 = {};
#define GPACE() do { SBAR(); asm volatile("s_nop 0"); SBAR(); } while (0)
    unsigned vo[8];
#pragma unroll
    for (int i = 0; i < 8; ++i) vo[i] = g < 2 ? voff[i & 1] + (i >> 1) * 65536 : voff[0] + (i >> 2) * 16384 + (i & 3) * 1024;
    const int nslot = g < 2 ? 4 : 3;
    const char* rp = src + (size_t)ahead * sstep;
    int rs = ahead;
    if (g < 2) WAITV(16); else WAITV(8);
    *fown = 1;
    flag_wait(*fall, fall, 1);
    asm volatile("" ::: "memory");
    bf16x8 xc0, xc1, xb0, xb1, yc0, yc1, yb0, yb1;
    int cs = 0, bs = 0;
#define GLD(S, cso, bso, ks) do { const lds_cptr cb_ = (lds_cptr)lds + (cso), bb_ = (lds_cptr)lds + bf + (bso); \
      S##b0 = ldsfrag(bb_ + (ks) * 4096); S##c0 = ldsfrag(cb_ + (cf0 ^ ((ks) << 5))); S##c1 = ldsfrag(cb_ + (cf1 ^ ((ks) << 5))); S##b1 = ldsfrag(bb_ + (ks) * 4096 + 1024); SBAR(); } while (0)
#define GPC(on, i) do { dma16_if(on, rp, vo[i], rd + (i) * 1024); SBAR(); } while (0)
    unsigned rd = dst + rs * 16384;
    GLD(x, 0, 0, 0);
    GPC(1, 0); GPC(1, 1);
#pragma unroll 1
    for (int m = 0; m < 15; ++m) {
      const int more = 16 - ahead - m;
#define GMM(acc, u, v) do { acc = MFMA(u, v, acc); GPACE(); } while (0)
      GLD(y, cs, bs, 1);
      GMM(a00, xb0, xc0); GMM(a01, xb0, xc1); GPC(more, 2); GMM(a10, xb1, xc0); GPC(more, 3); GMM(a11, xb1, xc1); GPC(more, 4);
      GLD(x, cs, bs, 2);
      GMM(a00, yb0, yc0); GPC(more, 5); GMM(a01, yb0, yc1); GPC(more, 6); GMM(a10, yb1, yc0); GPC(more, 7); GMM(a11, yb1, yc1);
      GLD(y, cs, bs, 3);
      GMM(a00, xb0, xc0); GMM(a01, xb0, xc1);
      if (g < 2) { if (m < 13) WAITV(16); else if (m == 13) WAITV(8); else WAITV(0); }
      else { if (m < 14) WAITV(8); else WAITV(0); }
      WAITL();
      *fown = m + 2;
      SBAR();
      GMM(a10, xb1, xc0); GMM(a11, xb1, xc1);
      a00 = MFMA(yb0, yc0, a00); GPACE();
      const unsigned fl = *fall;
      SBAR();
      a01 = MFMA(yb0, yc1, a01); GPACE();
      flag_wait(fl, fall, m + 2);
      asm volatile("" ::: "memory");
      cs = (cs + 16384) & 65535; bs = bs == 32768 ? 0 : bs + 16384;
      rp += sstep; rs = rs + 1 == nslot ? 0 : rs + 1; rd = dst + rs * 16384;
      GLD(x, cs, bs, 0);
      const int more1 = 15 - ahead - m;
      a10 = MFMA(yb1, yc0, a10); GPACE(); GPC(more1, 0);
      a11 = MFMA(yb1, yc1, a11); GPACE(); GPC(more1, 1);
    }
#define GM4N(S) do { a00 = MFMA(S##b0, S##c0, a00); a01 = MFMA(S##b0, S##c1, a01); a10 = MFMA(S##b1, S##c0, a10); a11 = MFMA(S##b1, S##c1, a11); SBAR(); } while (0)
    GLD(y, cs, bs, 1); GM4N(x);
    GLD(x, cs, bs, 2); GM4N(y);
    GLD(y, cs, bs, 3); GM4N(x);
    GM4N(y);
    const int tt = tb * 4 + 2 * tm, et = eb * 4 + 2 * en;
    const __amdgpu_buffer_rsrc_t wsr = mkrsrc(ws, WS_END);
    const unsigned pp = (unsigned)WS_P + (((ksp * 64 + tt) * 16 + et) << 11) + lane * 16;
#define ST2(off, v) __builtin_amdgcn_raw_buffer_store_b128(v, wsr, pp + (off), 0, DEV_AUX)
    ST2(0, pack8(a00, 0)); ST2(1024, pack8(a00, 8));
    ST2(2048, pack8(a10, 0)); ST2(2048 + 1024, pack8(a10, 8));
    ST2(32768, pack8(a01, 0)); ST2(32768 + 1024, pack8(a01, 8));
    ST2(32768 + 2048, pack8(a11, 0)); ST2(32768 + 2048 + 1024, pack8(a11, 8));
    WAITV(0);
    *fown = 17;
    gu32* const flags = (gu32*)(ws + WS_F);
    if (g == 0) {
      flag_wait(*fall, fall, 17);
      if (lane == 0) __hip_atomic_store(flags + blockIdx.x * 32, 1u, __ATOMIC_RELAXED, __HIP_MEMORY_SCOPE_AGENT);
    }
    {
      gu32* const pf = flags + (k * 8 + (lane & 3) * 2 + (x & 1)) * 32;
      for (unsigned spins = 0; spins < (1u << 20); ++spins) {
        const unsigned f = __hip_atomic_load(pf, __ATOMIC_RELAXED, __HIP_MEMORY_SCOPE_AGENT);
        if (__builtin_amdgcn_ballot_w64(f == 0) == 0) break;
        __builtin_amdgcn_s_sleep(0);
      }
    }
    __builtin_amdgcn_fence(__ATOMIC_ACQUIRE, "wavefront");
    {
      const int ttr = tb * 4 + ksp;
      const unsigned rp = (unsigned)WS_P + ((ttr * 16 + eb * 4 + g) << 11) + lane * 16;
      u32x4 pv[8];
#pragma unroll
      for (int i = 0; i < 8; ++i) pv[i] = __builtin_amdgcn_raw_buffer_load_b128(wsr, rp + (i >> 1) * (64 * 16 << 11) + (i & 1) * 1024, 0, DEV_AUX);
      f32x16 acc;
#pragma unroll
      for (int hf = 0; hf < 2; ++hf)
#pragma unroll
        for (int i = 0; i < 4; ++i) {
          acc[8 * hf + 2 * i] = (__uint_as_float(pv[hf][i] << 16) + __uint_as_float(pv[2 + hf][i] << 16)) + (__uint_as_float(pv[4 + hf][i] << 16) + __uint_as_float(pv[6 + hf][i] << 16));
          acc[8 * hf + 2 * i + 1] = (__uint_as_float(pv[hf][i] & 0xffff0000u) + __uint_as_float(pv[2 + hf][i] & 0xffff0000u)) + (__uint_as_float(pv[4 + hf][i] & 0xffff0000u) + __uint_as_float(pv[6 + hf][i] & 0xffff0000u));
        }
      const int row = lane & 31;
      LAS char* tp = (LAS char*)lds + K2_C + row * 256;
      *(LAS u32x4*)(tp + (((g * 4 + hi) ^ (row & 7)) << 4)) = natorder(pack8(acc, 0));
      *(LAS u32x4*)(tp + (((g * 4 + 2 + hi) ^ (row & 7)) << 4)) = natorder(pack8(acc, 8));
      WAITL();
      *fown = 18;
      flag_wait(*fall, fall, 18);
      asm volatile("" ::: "memory");
      const __amdgpu_buffer_rsrc_t outr = mkrsrc(out, 2048 * 2048);
#pragma unroll
      for (int i = 0; i < 2; ++i) {
        const int r = g * 8 + i * 4 + (lane >> 4);
        const u32x4 v = *(LAS const u32x4*)((LAS char*)lds + K2_C + r * 256 + (((lane & 15) ^ (r & 7)) << 4));
        __builtin_amdgcn_raw_buffer_store_b128(v, outr, (ttr * 32 + r) * 2048 + 1024 + eb * 256 + (lane & 15) * 16, 0, WT_AUX);
      }
    }
  }
}

constexpr int K3_O = 0, K3_W = 65536, K3_T = 131072, K3_LDS = K3_T + 8192;
extern "C" __global__ void __launch_bounds__(512, 2) k3_out(const char* __restrict__ wo, const char* __restrict__ ws_, char* __restrict__ out) {
  extern __shared__ __attribute__((aligned(16))) char lds[];
  char* ws = (char*)ws_;
  const int lane = threadIdx.x & 63, w = __builtin_amdgcn_readfirstlane(threadIdx.x >> 6), hi = lane >> 5;
  const int x = blockIdx.x & 7, k = blockIdx.x >> 3;
  const unsigned lds0 = (unsigned)(uintptr_t)lds;
  const int tb = 4 * x + (k >> 3), nb = k & 7;
  {
    const int r = lane >> 3;
    const unsigned vo = r * 1024 + (((lane & 7) ^ (((r >> 1) & 1) << 2)) << 4);
    const char* osrc = ws + WS_O + ((size_t)((tb * 2 + (w >> 2)) * 32 + 2 * (w & 3)) << 10);
    const unsigned odst = lds0 + K3_O + (w >> 2) * 32768 + (w & 3) * 2048;
    const char* wsrc = wo + __builtin_amdgcn_readfirstlane(nb * 128) + (size_t)w * 16 * 1024;
    const unsigned wdst = lds0 + K3_W + w * 2048;
#pragma unroll
    for (int c = 0; c < 4; ++c) {
      dma16(wsrc + (size_t)c * 128 * 1024, vo, wdst + c * 16384);
      dma16(osrc + c * 8192, lane * 16, odst + c * 8192);
      dma16(wsrc + (size_t)c * 128 * 1024 + 8 * 1024, vo, wdst + c * 16384 + 1024);
      dma16(osrc + c * 8192 + 1024, lane * 16, odst + c * 8192 + 1024);
    }
  }
  const int tm = w & 1, nn = (w >> 1) & 1;
  const int tt = tb * 2 + tm;
  f32x16 acc = {};
  if (w < 4) {
    const int q4 = (lane & 15) >> 2, p4 = lane & 3, blk = (lane >> 4) & 1;
    const lds_cptr wf = (lds_cptr)lds + K3_W + (4 * hi + q4) * 128 + (((4 * nn + 2 * blk + (p4 >> 1)) ^ ((q4 >> 1) << 2)) << 4) + (p4 & 1) * 8;
    const lds_cptr of = (lds_cptr)lds + K3_O + tm * 32768 + lane * 16;
#define K3CHUNK(c, N) do { WAIT_BAR(N); \
      bf16x8 a_[8], b_[8]; \
      _Pragma("unroll") for (int i = 0; i < 8; ++i) { a_[i] = cat8(ldstr(wf + ((c) * 8 + i) * 2048), ldstr(wf + ((c) * 8 + i) * 2048 + 1024)); b_[i] = ldsfrag(of + ((c) * 8 + i) * 1024); } \
      SBAR(); \
      _Pragma("unroll") for (int i = 0; i < 8; ++i) acc = MFMA(a_[i], b_[i], acc); SBAR(); } while (0)
    K3CHUNK(0, 12); K3CHUNK(1, 8); K3CHUNK(2, 4); K3CHUNK(3, 0);
  } else {
    WAIT_BAR(12); WAIT_BAR(8); WAIT_BAR(4); WAIT_BAR(0);
  }
  if (w < 4) {
    const int row = tm * 32 + (lane & 31);
    LAS char* tp = (LAS char*)lds + K3_T + row * 128;
    *(LAS u32x4*)(tp + (((nn * 4 + hi) ^ (row & 7)) << 4)) = natorder(pack8(acc, 0));
    *(LAS u32x4*)(tp + (((nn * 4 + 2 + hi) ^ (row & 7)) << 4)) = natorder(pack8(acc, 8));
  }
  asm volatile("s_waitcnt lgkmcnt(0)\n\ts_barrier" ::: "memory");
  {
    const __amdgpu_buffer_rsrc_t outr = mkrsrc(out, 2048 * 2048);
    const int row = w * 8 + (lane >> 3);
    const u32x4 v = *(LAS const u32x4*)((LAS char*)lds + K3_T + row * 128 + (((lane & 7) ^ (row & 7)) << 4));
    __builtin_amdgcn_raw_buffer_store_b128(v, outr, (tb * 64 + row) * 2048 + nb * 128 + (lane & 7) * 16, 0, WT_AUX);
  }
}

constexpr int K1_SLOT = 40960, K1_LDS = 4 * K1_SLOT;
#define FLAG_CLEAR() do { if (threadIdx.x == 511) __hip_atomic_store((gu32*)(ws + WS_F) + blockIdx.x * 32, 0u, __ATOMIC_RELAXED, __HIP_MEMORY_SCOPE_AGENT); } while (0)
#define CODE_PREFETCH() do { FLAG_CLEAR(); if (p == 0) { \
      unsigned long long c0, c1; \
      asm volatile("s_getpc_b64 s[40:41]\n\ts_add_u32 s40, s40, k2_attn@rel32@lo+4\n\ts_addc_u32 s41, s41, k2_attn@rel32@hi+12\n\ts_mov_b64 %0, s[40:41]" : "=s"(c0) :: "s40", "s41", "scc"); \
      asm volatile("s_getpc_b64 s[40:41]\n\ts_add_u32 s40, s40, k1_proj@rel32@lo+4\n\ts_addc_u32 s41, s41, k1_proj@rel32@hi+12\n\ts_mov_b64 %0, s[40:41]" : "=s"(c1) :: "s40", "s41", "scc"); \
      const unsigned long long a = c0 + k * 1024 + lane * 16; \
      if (a + 16 <= c1) { const u32x4 t_ = *(const __attribute__((address_space(1))) u32x4*)a; asm volatile("" :: "v"(t_)); } \
    } } while (0)
extern "C" __global__ void __launch_bounds__(512, 2) k1_proj(const char* __restrict__ xa, const char* __restrict__ wqkv, const char* __restrict__ xb,
                                                  const char* __restrict__ dre, const char* __restrict__ dim, char* __restrict__ ws) {
  extern __shared__ __attribute__((aligned(16))) char lds[];
  const int lane = threadIdx.x & 63, w = __builtin_amdgcn_readfirstlane(threadIdx.x >> 6), hi = lane >> 5;
  const int x = blockIdx.x & 7, k = blockIdx.x >> 3;
  const int rb = (x & 3) * 4 + (k & 3), cb = (x >> 2) * 4 + ((k >> 2) & 3);
  const bool qkv = k < 16;
  const unsigned lds0 = (unsigned)(uintptr_t)lds;
  if (__builtin_expect(w >= 4, 1)) {
    const int p = w - 4;
    const int rot = (k + x) & 7;
    unsigned vx[2];
#pragma unroll
    for (int j = 0; j < 2; ++j) { const int row = 8 * j + (lane >> 3); vx[j] = row * 1024 + (((lane & 7) ^ ((row >> 1) & 7)) << 4); }
    if (__builtin_expect(qkv, 1)) {
      const char* xs = xa + (size_t)(rb * 128 + p * 32) * 1024;
      const char* wsrc = wqkv + cb * 128 + (size_t)p * 16 * 3072;
      const int wr = lane >> 3;
      const unsigned vw = wr * 3072 + (((lane & 7) ^ (((wr >> 1) & 1) << 2)) << 4);
      auto issue = [&](int s) {
        const int ks = (s + rot) & 7;
        const unsigned sl = lds0 + (s & 3) * K1_SLOT;
        const char* xp = xs + ks * 128;
        const char* wp = wsrc + (size_t)ks * 64 * 3072;
#pragma unroll
        for (int i = 0; i < 4; ++i) dma16(xp + (i >> 1) * 16384, vx[i & 1], sl + p * 4096 + i * 1024);
#pragma unroll
        for (int i = 0; i < 6; ++i) dma16(wp + (i >> 1) * 1024 + (i & 1) * 8 * 3072, vw, sl + 16384 + (i >> 1) * 8192 + p * 2048 + (i & 1) * 1024);
      };
#pragma unroll 1
      for (int i = 0; i < 10; ++i) {
        if (i >= 2) {
          if (i < 9) WAITV(10); else WAITV(0);
          BAR();
        }
        if (i < 8) issue(i);
      }
      CODE_PREFETCH();
      return;
    }
    {
      const char* xs = xb + (size_t)(rb * 128 + p * 32) * 1024;
      const char* ds = ((cb >> 2) ? dim : dre) + (size_t)((cb & 3) * 128 + p * 32) * 1024;
      auto issue = [&](int s) {
        const int ks = (s + rot) & 7;
        const unsigned sl = lds0 + (s & 3) * K1_SLOT;
#pragma unroll
        for (int i = 0; i < 4; ++i) dma16(xs + ks * 128 + (i >> 1) * 16384, vx[i & 1], sl + p * 4096 + i * 1024);
#pragma unroll
        for (int i = 0; i < 4; ++i) dma16(ds + ks * 128 + (i >> 1) * 16384, vx[i & 1], sl + 16384 + p * 4096 + i * 1024);
      };
#pragma unroll 1
      for (int i = 0; i < 10; ++i) {
        if (i >= 2) {
          if (i < 9) WAITV(8); else WAITV(0);
          BAR();
        }
        if (i < 8) issue(i);
      }
    }
    CODE_PREFETCH();
    return;
  }
  const __amdgpu_buffer_rsrc_t wsr = mkrsrc(ws, WS_P);
  const int tm = w & 1, ch = w >> 1;
  const int frow = 64 * tm + (lane & 31);
  const unsigned xa0 = lds0 + frow * 128 + ((hi ^ ((frow >> 1) & 7)) << 4);
  auto ldx = [&](unsigned so, int ks, int t) { return ldsfrag((lds_cptr)(uintptr_t)((xa0 ^ (ks << 5)) + so) + t * 4096); };
  if (qkv) {
    const int q4 = (lane & 15) >> 2, p4 = lane & 3, blk = (lane >> 4) & 1;
    const unsigned wa0 = lds0 + 16384 + (8 * hi + q4) * 128 + (((4 * ch + 2 * blk + (p4 >> 1)) ^ ((q4 >> 1) << 2)) << 4) + (p4 & 1) * 8;
    auto ldw = [&](unsigned so, int ks, int m) { const lds_cptr wp = (lds_cptr)(uintptr_t)(wa0 + so) + ks * 2048 + m * 8192; return cat8(ldstr(wp), ldstr(wp + 512)); };
    f32x16 aq0 = {}, aq1 = {}, ak0 = {}, ak1 = {}, av0 = {}, av1 = {};
    bf16x8 x0, x1, wq, wk, wv, nx0, nx1, nwq, nwk, nwv;
    BAR();
    x0 = ldx(0, 0, 0); wq = ldw(0, 0, 0); x1 = ldx(0, 0, 1); wk = ldw(0, 0, 1); wv = ldw(0, 0, 2);
#define QSUB(so, ks, SYNC, LOAD) do { \
      SBAR(); \
      aq0 = MFMA(wq, x0, aq0); SBAR(); \
      if (SYNC) BAR(); SBAR(); \
      if (LOAD) { nx0 = ldx(so, ks, 0); nx1 = ldx(so, ks, 1); } SBAR(); \
      aq1 = MFMA(wq, x1, aq1); SBAR(); \
      if (LOAD) { nwq = ldw(so, ks, 0); } SBAR(); \
      ak0 = MFMA(wk, x0, ak0); SBAR(); \
      if (LOAD) { nwk = ldw(so, ks, 1); } SBAR(); \
      ak1 = MFMA(wk, x1, ak1); SBAR(); \
      if (LOAD) { nwv = ldw(so, ks, 2); } SBAR(); \
      av0 = MFMA(x0, wv, av0); SBAR(); \
      av1 = MFMA(x1, wv, av1); SBAR(); \
      if (LOAD) { x0 = nx0; x1 = nx1; wq = nwq; wk = nwk; wv = nwv; } \
    } while (0)
#pragma unroll 1
    for (int s = 0; s < 7; ++s) {
      const unsigned so = (s & 3) * K1_SLOT, sn = ((s + 1) & 3) * K1_SLOT;
      QSUB(so, 1, false, true); QSUB(so, 2, false, true); QSUB(so, 3, false, true);
      QSUB(sn, 0, true, true);
    }
    constexpr float QS = 0.25f * 1.4426950408889634f;
    const int h0 = 4 * cb + 2 * ch, tile = rb * 4 + 2 * tm;
    const unsigned qo = WS_Q + ((h0 * 64 + tile) << 10) + lane * 16;
    const unsigned ko = qo + (unsigned)(WS_K - WS_Q);
    const unsigned vo = WS_V + ((h0 + ((lane >> 4) & 1)) << 16) + tile * 1024 + (hi * 16 + (lane & 15)) * 16;
    {
      constexpr unsigned so = 3 * K1_SLOT;
#define TM(acc, u, v) do { acc = MFMA(u, v, acc); SBAR(); } while (0)
#define TS(off, val) do { st16(wsr, off, val); SBAR(); } while (0)
      SBAR();
      TM(aq0, wq, x0); const bf16x8 xa1 = ldx(so, 1, 0), xb1 = ldx(so, 1, 1); SBAR();
      TM(aq1, wq, x1); const bf16x8 q1 = ldw(so, 1, 0); SBAR();
      TM(ak0, wk, x0); const bf16x8 xa2 = ldx(so, 2, 0), xb2 = ldx(so, 2, 1); SBAR();
      TM(ak1, wk, x1); const bf16x8 q2 = ldw(so, 2, 0); SBAR();
      TM(av0, x0, wv); const bf16x8 xa3 = ldx(so, 3, 0), xb3 = ldx(so, 3, 1); SBAR();
      TM(av1, x1, wv); const bf16x8 q3 = ldw(so, 3, 0); SBAR();
      TM(aq0, q1, xa1); const bf16x8 k1 = ldw(so, 1, 1); SBAR();
      TM(aq1, q1, xb1);
      TM(aq0, q2, xa2); const bf16x8 k2 = ldw(so, 2, 1); SBAR();
      TM(aq1, q2, xb2);
      TM(aq0, q3, xa3); const bf16x8 k3 = ldw(so, 3, 1); SBAR();
      TM(aq1, q3, xb3);
      TM(ak0, k1, xa1); const bf16x8 v1 = ldw(so, 1, 2); SBAR();
      TM(ak1, k1, xb1); const bf16x8 v2 = ldw(so, 2, 2); SBAR(); TS(qo, pack8(aq0, 0, QS));
      TM(ak0, k2, xa2); const bf16x8 v3 = ldw(so, 3, 2); SBAR(); TS(qo + 65536, pack8(aq0, 8, QS));
      TM(ak1, k2, xb2); TS(qo + 1024, pack8(aq1, 0, QS));
      TM(ak0, k3, xa3); TS(qo + 1024 + 65536, pack8(aq1, 8, QS));
      TM(ak1, k3, xb3);
      TM(av0, xa1, v1);
      TM(av1, xb1, v1); TS(ko, pack8(ak0, 0));
      TM(av0, xa2, v2); TS(ko + 65536, pack8(ak0, 8));
      TM(av1, xb2, v2); TS(ko + 1024, pack8(ak1, 0));
      TM(av0, xa3, v3); TS(ko + 1024 + 65536, pack8(ak1, 8));
      TM(av1, xb3, v3);
    }
    st16(wsr, vo, pack8(av0, 0)); st16(wsr, vo + 512, pack8(av0, 8));
    st16(wsr, vo + 1024, pack8(av1, 0)); st16(wsr, vo + 1024 + 512, pack8(av1, 8));
  } else {
    const int er = 64 * ch + (lane & 31);
    const unsigned da0 = lds0 + 16384 + er * 128 + ((hi ^ ((er >> 1) & 7)) << 4);
    auto ldd = [&](unsigned so, int ks, int t) { return ldsfrag((lds_cptr)(uintptr_t)((da0 ^ (ks << 5)) + so) + t * 4096); };
    f32x16 a00 = {}, a01 = {}, a10 = {}, a11 = {};
    bf16x8 x0, x1, d0, d1, nx0, nx1, nd0, nd1;
    BAR();
    x0 = ldx(0, 0, 0); d0 = ldd(0, 0, 0); d1 = ldd(0, 0, 1); x1 = ldx(0, 0, 1);
#define DSUB(so, ks, SYNC, LOAD) do { \
      SBAR(); \
      a00 = MFMA(x0, d0, a00); SBAR(); \
      if (SYNC) BAR(); SBAR(); \
      if (LOAD) { nx0 = ldx(so, ks, 0); } SBAR(); \
      a01 = MFMA(x0, d1, a01); SBAR(); \
      if (LOAD) { nd0 = ldd(so, ks, 0); } SBAR(); \
      a10 = MFMA(x1, d0, a10); SBAR(); \
      if (LOAD) { nd1 = ldd(so, ks, 1); } SBAR(); \
      a11 = MFMA(x1, d1, a11); SBAR(); \
      if (LOAD) { nx1 = ldx(so, ks, 1); } SBAR(); \
      if (LOAD) { x0 = nx0; x1 = nx1; d0 = nd0; d1 = nd1; } \
    } while (0)
#pragma unroll 1
    for (int s = 0; s < 7; ++s) {
      const unsigned so = (s & 3) * K1_SLOT, sn = ((s + 1) & 3) * K1_SLOT;
      DSUB(so, 1, false, true); DSUB(so, 2, false, true); DSUB(so, 3, false, true);
      DSUB(sn, 0, true, true);
    }
    DSUB(3 * K1_SLOT, 1, false, true); DSUB(3 * K1_SLOT, 2, false, true); DSUB(3 * K1_SLOT, 3, false, true);
    DSUB(0, 0, false, false);
    const int part = cb >> 2;
    const float sg = part ? -1.f : 1.f;
    const int et = (cb & 3) * 4 + 2 * ch, s16 = rb * 8 + tm * 4;
    const unsigned op = WS_AB + (((part * 128 + s16) * 16 + et) << 10) + lane * 16;
    st16(wsr, op, natorder(pack8(a00, 0, sg))); st16(wsr, op + 16384, natorder(pack8(a00, 8, sg)));
    st16(wsr, op + 1024, natorder(pack8(a01, 0, sg))); st16(wsr, op + 1024 + 16384, natorder(pack8(a01, 8, sg)));
    st16(wsr, op + 32768, natorder(pack8(a10, 0, sg))); st16(wsr, op + 32768 + 16384, natorder(pack8(a10, 8, sg)));
    st16(wsr, op + 32768 + 1024, natorder(pack8(a11, 0, sg))); st16(wsr, op + 32768 + 1024 + 16384, natorder(pack8(a11, 8, sg)));
  }
}

extern "C" void kernel_launch(void* const* d_in, const int* in_sizes, int n_in, void* d_out, int out_size, void* d_ws, size_t ws_size, hipStream_t stream) {
  const char* xa = (const char*)d_in[0];
  const char* wqkv = (const char*)d_in[1];
  const char* wo = (const char*)d_in[2];
  const char* xb = (const char*)d_in[3];
  const char* cre = (const char*)d_in[4];
  const char* cim = (const char*)d_in[5];
  const char* dre = (const char*)d_in[6];
  const char* dim = (const char*)d_in[7];
  char* ws = (char*)d_ws;
  hipFuncSetAttribute((const void*)k1_proj, hipFuncAttributeMaxDynamicSharedMemorySize, K1_LDS);
  hipFuncSetAttribute((const void*)k2_attn, hipFuncAttributeMaxDynamicSharedMemorySize, K2_LDS);
  hipFuncSetAttribute((const void*)k3_out, hipFuncAttributeMaxDynamicSharedMemorySize, K3_LDS);
  k1_proj<<<256, 512, K1_LDS, stream>>>(xa, wqkv, xb, dre, dim, ws);
  k2_attn<<<256, K2_THREADS, K2_LDS, stream>>>(cre, cim, ws, wo, (char*)d_out);
  k3_out<<<256, 512, K3_LDS, stream>>>(wo, ws, (char*)d_out);
}
```
